```python
import torch

def _softmax(x, axis=-1):
    return torch.softmax(x, dim=axis)
def _gelu(x):
    return torch.nn.functional.gelu(x, approximate="tanh")
def _silu(x):
    return torch.nn.functional.silu(x)
def _sigmoid(x):
    return torch.sigmoid(x)
def _layernorm(x, axis=-1, eps=1e-5):
    m = x.mean(dim=axis, keepdim=True)
    v = x.var(dim=axis, keepdim=True, unbiased=False)
    return (x - m) / torch.sqrt(v + eps)
def _scatter_add(tgt, idx, src, axis=0):
    return tgt.clone().index_add_(axis, idx, src)
def _segment_sum(src, seg, n):
    out = torch.zeros((n,) + src.shape[1:], dtype=src.dtype)
    return out.index_add_(0, seg, src)
def _pad(x, pw):
    flat = tuple(v for pair in reversed(pw) for v in pair)
    return torch.nn.functional.pad(x, flat)

kernel_name = 'recurrent_scan__softmax_full_mha__glu_gated_glu__prompt'

def setup_inputs():

    _w = (lambda a: (lambda t: t.to(torch.bfloat16) if t.is_floating_point() else t)(torch.as_tensor(a)))
    def _A_setup():
        B, S, D = 1, 2048, 576
        return {
            "x": _w((lambda *s: torch.randn(*s, dtype=torch.bfloat16) * 0.05)(B, S, D)),
            "wqkv": _w((lambda *s: torch.randn(*s, dtype=torch.bfloat16) * 0.05)(D, 3 * D)),
            "wo": _w((lambda *s: torch.randn(*s, dtype=torch.bfloat16) * 0.05)(D, D)),
        }
    _A_in = _A_setup()

    def _B_setup():
        B, S, D, F = 1, 2048, 576, 4608
        return {
            "x": _w((lambda *s: torch.randn(*s, dtype=torch.bfloat16) * 0.05)(B, S, D)),
            "w_gate": _w((lambda *s: torch.randn(*s, dtype=torch.bfloat16) * 0.05)(D, F)),
            "w_up": _w((lambda *s: torch.randn(*s, dtype=torch.bfloat16) * 0.05)(D, F)),
            "w_down": _w((lambda *s: torch.randn(*s, dtype=torch.bfloat16) * 0.05)(F, D)),
        }
    _B_in = _B_setup()

    return {
        "A_x": _A_in["x"],
        "A_wqkv": _A_in["wqkv"],
        "A_wo": _A_in["wo"],
        "B_x": _B_in["x"],
        "B_w_gate": _B_in["w_gate"],
        "B_w_up": _B_in["w_up"],
        "B_w_down": _B_in["w_down"],
    }

def reference(A_x, A_wqkv, A_wo, B_x, B_w_gate, B_w_up, B_w_down):
    def _A_ref(x, wqkv, wo):
        B, S, D = x.shape
        H = 32; Dh = D // H
        qkv = (lambda x, *d: x.permute(*d))((x @ wqkv).reshape(B, S, 3, H, Dh), 2, 0, 3, 1, 4)
        q, k, v = qkv[0], qkv[1], qkv[2]
        scale = Dh ** -0.5
        scores = torch.einsum("bhqd,bhkd->bhqk", q, k) * scale
        p = _softmax(scores, axis=-1)
        o = torch.einsum("bhqk,bhkd->bhqd", p, v)
        return (lambda x, *d: x.permute(*d))(o, 0, 2, 1, 3).reshape(B, S, D) @ wo

    def _B_ref(x, w_gate, w_up, w_down):
        g = torch.einsum("bsd,df->bsf", x, w_gate)
        u = torch.einsum("bsd,df->bsf", x, w_up)
        h = _sigmoid(g) * u
        return torch.einsum("bsf,fd->bsd", h, w_down)

    x = A_x
    xn = _layernorm(x, axis=-1)
    s = _A_ref(xn, A_wqkv, A_wo)
    g = _silu(_B_ref(xn, B_w_gate, B_w_up, B_w_down))
    return x + s * g
```

```cpp
#include <hip/hip_runtime.h>
#include <hip/hip_bf16.h>
#include <stdint.h>

typedef __attribute__((ext_vector_type(8))) short bf16x8;
typedef __attribute__((ext_vector_type(4))) short bf16x4;
typedef __attribute__((ext_vector_type(16))) float f32x16;
typedef __attribute__((ext_vector_type(4))) float f32x4;
typedef __attribute__((ext_vector_type(4))) unsigned u32x4;
typedef __attribute__((ext_vector_type(2))) unsigned u32x2;
typedef __attribute__((address_space(3))) char lds_char;
typedef unsigned short u16;

constexpr int S_ = 2048, D_ = 576, NH = 32, DH = 18, F_ = 4608;
constexpr int NTT = S_ / 32;
constexpr int NKS = D_ / 16;
constexpr int NKB = F_ / 16;
constexpr float QSCALE = 0.23570226039551584f * 1.4426950408889634f;
constexpr float LOG2E = 1.4426950408889634f;

constexpr size_t WS_XN  = 0;
constexpr size_t WS_WOF = WS_XN + (size_t)NTT * NKS * 1024;
constexpr size_t WS_QM  = WS_WOF + (size_t)18 * NKS * 1024;
constexpr size_t WS_KM  = WS_QM + (size_t)NH * NTT * 1024;
constexpr size_t WS_QT  = WS_KM + (size_t)NH * NTT * 1024;
constexpr size_t WS_KT  = WS_QT + (size_t)NH * 2 * S_ * 2;
constexpr size_t WS_VF  = WS_KT + (size_t)NH * 2 * S_ * 2;
constexpr size_t WS_H   = WS_VF + (size_t)NH * NTT * 1280;
constexpr size_t WS_OF  = WS_H + (size_t)NTT * NKB * 1024;
constexpr size_t WS_PD  = WS_OF + (size_t)NTT * NKS * 1024;
constexpr size_t WS_END = WS_PD + (size_t)4 * NTT * 18 * 2048;

#define MFMA32(a, b, c) __builtin_amdgcn_mfma_f32_32x32x16_bf16(a, b, c, 0, 0, 0)
#define MFMA32K8(a, b, c) __builtin_amdgcn_mfma_f32_32x32x8bf16_1k(a, b, c, 0, 0, 0)
#define SBAR() __builtin_amdgcn_sched_barrier(0)

__device__ __forceinline__ unsigned cvtpk(float lo, float hi) {
    typedef __attribute__((ext_vector_type(2))) float f2_t;
    typedef __attribute__((ext_vector_type(2))) __bf16 b2_t;
    return __builtin_bit_cast(unsigned, __builtin_convertvector((f2_t){lo, hi}, b2_t));
}
__device__ __forceinline__ float bf2f(u16 v) { return __uint_as_float((unsigned)v << 16); }
__device__ __forceinline__ float bflo(unsigned v) { return __uint_as_float(v << 16); }
__device__ __forceinline__ float bfhi(unsigned v) { return __uint_as_float(v & 0xffff0000u); }
__device__ __forceinline__ float rbf(float v) { return bflo(cvtpk(v, v)); }
__device__ __forceinline__ float ex2(float v) { return __builtin_amdgcn_exp2f(v); }
__device__ __forceinline__ float rcp(float v) { return __builtin_amdgcn_rcpf(v); }

__device__ __forceinline__ void glds16(const void* sbase, unsigned voff, unsigned lds_base) {
    unsigned sv;
    asm volatile("s_mov_b32 %0, m0\n\ts_mov_b32 m0, %3\n\ts_nop 0\n\tglobal_load_lds_dwordx4 %1, %2\n\ts_mov_b32 m0, %0"
                 : "=&s"(sv) : "v"(voff), "s"(sbase), "s"(lds_base) : "memory");
}
__device__ __forceinline__ void glds4(const void* sbase, unsigned voff, unsigned lds_base) {
    unsigned sv;
    asm volatile("s_mov_b32 %0, m0\n\ts_mov_b32 m0, %3\n\ts_nop 0\n\tglobal_load_lds_dword %1, %2\n\ts_mov_b32 m0, %0"
                 : "=&s"(sv) : "v"(voff), "s"(sbase), "s"(lds_base) : "memory");
}
typedef short v4i16_t __attribute__((ext_vector_type(4)));
__device__ __forceinline__ bf16x4 trread(const lds_char* p) {
    return __builtin_bit_cast(bf16x4, __builtin_amdgcn_ds_read_tr16_b64_v4i16((__attribute__((address_space(3))) v4i16_t*)p));
}
__device__ __forceinline__ bf16x8 wfrag(const lds_char* p, int off0, int off1) {
    bf16x4 a = trread(p + off0), b = trread(p + off1);
    return (bf16x8){a[0], a[1], a[2], a[3], b[0], b[1], b[2], b[3]};
}
__device__ __forceinline__ int colperm(int rho) { const int p = rho >> 2; return (((p & 1) << 3) | ((p >> 1) << 2)) + (rho & 3); }

__global__ __launch_bounds__(128) void k0_ln(const u16* __restrict__ x, const u16* __restrict__ wo, char* __restrict__ ws) {
    const int lane = threadIdx.x & 63;
    if (threadIdx.x < 64) {
        const int row = blockIdx.x * 8 + (lane >> 3), c = lane & 7;
        u32x4 v[9];
#pragma unroll
        for (int i = 0; i < 9; ++i) v[i] = *(const u32x4*)(x + (size_t)row * D_ + (c + 8 * i) * 8);
        float s = 0.f;
#pragma unroll
        for (int i = 0; i < 9; ++i)
#pragma unroll
            for (int j = 0; j < 4; ++j) s += bflo(v[i][j]) + bfhi(v[i][j]);
        s += __shfl_xor(s, 1); s += __shfl_xor(s, 2); s += __shfl_xor(s, 4);
        const float mean = s * (1.0f / D_);
        float q = 0.f;
#pragma unroll
        for (int i = 0; i < 9; ++i)
#pragma unroll
            for (int j = 0; j < 4; ++j) { const float a = bflo(v[i][j]) - mean, b = bfhi(v[i][j]) - mean; q += a * a + b * b; }
        q += __shfl_xor(q, 1); q += __shfl_xor(q, 2); q += __shfl_xor(q, 4);
        const float mb = rbf(mean), var = rbf(q * (1.0f / D_));
        const float sd = rbf(sqrtf(rbf(var + 1e-5f)));
        const int tt = row >> 5, r = row & 31;
#pragma unroll
        for (int i = 0; i < 9; ++i) {
            u32x4 o;
#pragma unroll
            for (int j = 0; j < 4; ++j) {
                const unsigned d = cvtpk(bflo(v[i][j]) - mb, bfhi(v[i][j]) - mb);
                o[j] = cvtpk(bflo(d) / sd, bfhi(d) / sd);
            }
            const int qd = c + 8 * i, ks = qd >> 1, hh = qd & 1;
            *(u32x4*)(ws + WS_XN + ((size_t)(tt * NKS + ks) * 64 + hh * 32 + r) * 16) = o;
        }
    } else {
        const int r = lane & 31, hh = lane >> 5;
        for (int f = blockIdx.x; f < 18 * NKS; f += gridDim.x) {
            const int nt = f / NKS, ks = f - nt * NKS;
            const int n = 32 * nt + (r & 16) + colperm(r & 15);
            u16 e[8];
#pragma unroll
            for (int j = 0; j < 8; ++j) {
                const int krow = ks < 32 ? ks * DH + 8 * hh + j : (8 * (ks - 32) + 4 * hh + (j >> 1)) * DH + 16 + (j & 1);
                e[j] = wo[(size_t)krow * D_ + n];
            }
            u32x4 o = {e[0] | ((unsigned)e[1] << 16), e[2] | ((unsigned)e[3] << 16), e[4] | ((unsigned)e[5] << 16), e[6] | ((unsigned)e[7] << 16)};
            *(u32x4*)(ws + WS_WOF + ((size_t)f * 64 + lane) * 16) = o;
        }
    }
}

constexpr int K1_UNIT = 73728, K1_LDS = 2 * K1_UNIT;

__device__ __forceinline__ f32x16 k1_tile(const lds_char* wp, const bf16x8 (&xf)[NKS], bool swapped) {
    f32x16 acc = {};
#pragma unroll
    for (int ks = 0; ks < NKS; ++ks) {
        const int off = (ks >> 2) * 8192 + (ks & 3) * 1024;
        const bf16x8 w = (ks < 20) ? wfrag(wp, off, off + 256) : wfrag(wp + 40960, off - 40960, off - 40960 + 256);
        acc = swapped ? MFMA32(xf[ks], w, acc) : MFMA32(w, xf[ks], acc);
    }
    return acc;
}

__global__ __launch_bounds__(512, 2) void k1_proj(const u16* __restrict__ wqkv, const u16* __restrict__ wg, const u16* __restrict__ wu,
                                                  char* __restrict__ ws) {
    extern __shared__ __attribute__((aligned(16))) char smem[];
    const lds_char* lds = (const lds_char*)smem;
    const unsigned lds0 = (unsigned)(uintptr_t)lds;
    const int tid = threadIdx.x, lane = tid & 63, r = lane & 31, hh = lane >> 5;
    const int w = __builtin_amdgcn_readfirstlane(tid >> 6);
    const int xcd = blockIdx.x & 7, idx = blockIdx.x >> 3;
    const int panel = (xcd & 1) * 4 + (idx & 3), j = (xcd >> 1) * 8 + (idx >> 2);
    const int tt = panel * 8 + w;

    const int srow = lane >> 2, scp = lane & 3;
    const u16* wsrc = (w < 4 ? wg : wu) + 144 * j;
    const int schunk = (w < 4) ? scp : (scp ^ 2);
    const unsigned voff_full = (unsigned)((16 * (w & 3) + srow) * (F_ * 2) + schunk * 16);
    const unsigned voff_half = (unsigned)((16 * (w & 3) + srow) * (F_ * 2) + (schunk & 1) * 16);
    const unsigned sdst = lds0 + (w >> 2) * 4096 + (w & 3) * 1024;
    auto stage_gu = [&](int u, int half) {
        const unsigned vo = (u == 4) ? voff_half : voff_full;
#pragma unroll
        for (int s = 0; s < 9; ++s)
            glds16((const char*)(wsrc + 32 * u) + (size_t)s * 64 * (F_ * 2), vo, sdst + half * K1_UNIT + s * 8192);
    };
    auto stage_qkv = [&](int half) {
        const int dw = lane & 15, rr = lane >> 4;
        int col;
        if (w < 4) col = dw < 8 ? j * DH + 2 * dw : 2 * D_ + j * DH + 2 * (dw - 8);
        else if (dw >= 8) col = D_ + j * DH + 2 * (dw - 8);
        else col = dw == 4 ? j * DH + 16 : dw == 5 ? D_ + j * DH + 16 : 2 * D_ + j * DH + 16;
        const unsigned vo = (unsigned)((16 * (w & 3) + rr) * (3 * D_ * 2) + col * 2);
#pragma unroll
        for (int s = 0; s < 9; ++s)
#pragma unroll
            for (int i = 0; i < 4; ++i)
                glds4((const char*)wqkv + (size_t)(s * 64 + 4 * i) * (3 * D_ * 2), vo, sdst + half * K1_UNIT + s * 8192 + i * 256);
    };

    bf16x8 xf[NKS];
#pragma unroll
    for (int ks = 0; ks < NKS; ++ks) xf[ks] = *(const bf16x8*)(ws + WS_XN + ((size_t)(tt * NKS + ks) * 64 + lane) * 16);
    stage_gu(0, 0);

    const int grp = (lane >> 4) & 1, q4 = (lane & 15) >> 2, p4 = lane & 3;
    const int rd_base = grp * 4096 + (8 * hh + q4) * 64 + (((p4 & 1) << 4) | ((p4 >> 1) << 3));
    const lds_char* wp0 = lds + rd_base + 32 * (0 ^ grp);
    const lds_char* wp1 = lds + rd_base + 32 * (1 ^ grp);

    auto glu_store = [&](const f32x16& a, int kb) {
        float hv[8];
#pragma unroll
        for (int i = 0; i < 8; ++i) hv[i] = a[8 + i] * rcp(1.0f + ex2(-LOG2E * a[i]));
        u32x4 o = {cvtpk(hv[0], hv[1]), cvtpk(hv[2], hv[3]), cvtpk(hv[4], hv[5]), cvtpk(hv[6], hv[7])};
        *(u32x4*)(ws + WS_H + ((size_t)(tt * NKB + kb) * 64 + lane) * 16) = o;
    };

#pragma unroll 1
    for (int u = 0; u < 5; ++u) {
        const int half = u & 1;
        asm volatile("s_waitcnt vmcnt(0)\n\ts_barrier" ::: "memory");
        if (u < 4) stage_gu(u + 1, half ^ 1); else stage_qkv(half ^ 1);
        {
            f32x16 a = k1_tile(wp0 + half * K1_UNIT, xf, false);
            glu_store(a, 9 * j + 2 * u);
        }
        if (u < 4) {
            f32x16 a = k1_tile(wp1 + half * K1_UNIT, xf, false);
            glu_store(a, 9 * j + 2 * u + 1);
        }
    }
    asm volatile("s_waitcnt vmcnt(0)\n\ts_barrier" ::: "memory");
    {
        f32x16 a = k1_tile(wp0 + K1_UNIT, xf, false);
        u32x4 oq = {cvtpk(a[0] * QSCALE, a[1] * QSCALE), cvtpk(a[2] * QSCALE, a[3] * QSCALE),
                    cvtpk(a[4] * QSCALE, a[5] * QSCALE), cvtpk(a[6] * QSCALE, a[7] * QSCALE)};
        u32x4 ok = {cvtpk(a[8], a[9]), cvtpk(a[10], a[11]), cvtpk(a[12], a[13]), cvtpk(a[14], a[15])};
        *(u32x4*)(ws + WS_QM + ((size_t)(j * NTT + tt) * 64 + lane) * 16) = oq;
        *(u32x4*)(ws + WS_KM + ((size_t)(j * NTT + tt) * 64 + lane) * 16) = ok;
    }
    {
        f32x16 a = k1_tile(wp1 + K1_UNIT, xf, true);
        const float sc = (r == 20 || r == 21) ? QSCALE : 1.0f;
        unsigned pk[8];
#pragma unroll
        for (int i = 0; i < 8; ++i) pk[i] = (r == 18) ? 0x3F803F80u : cvtpk(a[2 * i] * sc, a[2 * i + 1] * sc);
        if (r <= 18) {
            char* vb = ws + WS_VF + (size_t)(j * NTT + tt) * 1280 + hh * 320 + r * 16;
            *(u32x4*)(vb) = (u32x4){pk[0], pk[1], pk[2], pk[3]};
            *(u32x4*)(vb + 640) = (u32x4){pk[4], pk[5], pk[6], pk[7]};
        } else if (r >= 20 && r < 24) {
            char* tb = ws + (r < 22 ? WS_QT : WS_KT) + ((size_t)(j * 2 + (r & 1)) * S_ + tt * 32 + 4 * hh) * 2;
#pragma unroll
            for (int g = 0; g < 4; ++g) *(u32x2*)(tb + g * 16) = (u32x2){pk[2 * g], pk[2 * g + 1]};
        }
    }
}

__device__ __forceinline__ void store_o(char* ws, int head, int tq, int lane, const f32x16& o) {
    const int r = lane & 31, hh = lane >> 5;
    const float l = __shfl(o[10], r);
    const float inv = 1.0f / l;
    u32x4 m = {cvtpk(o[0] * inv, o[1] * inv), cvtpk(o[2] * inv, o[3] * inv), cvtpk(o[4] * inv, o[5] * inv), cvtpk(o[6] * inv, o[7] * inv)};
    *(u32x4*)(ws + WS_OF + ((size_t)(tq * NKS + head) * 64 + lane) * 16) = m;
    if (hh == 0)
        *(unsigned*)(ws + WS_OF + ((size_t)(tq * NKS + 32 + (head >> 3)) * 64 + r + 32 * ((head >> 2) & 1)) * 16 + (head & 3) * 4) =
            cvtpk(o[8] * inv, o[9] * inv);
}

__device__ __forceinline__ void attn_role(char* __restrict__ ws, char* smem, int bid) {
    const int tid = threadIdx.x, lane = tid & 63, r = lane & 31, hh = lane >> 5;
    const int w = __builtin_amdgcn_readfirstlane(tid >> 6);
    const int head = (bid & 7) * 4 + ((bid >> 3) & 3), qb = bid >> 5;
    const int qg = w & 1, kh = w >> 1;
    const char* kmb = ws + WS_KM + (size_t)head * NTT * 1024 + lane * 16;
    const u16* ktb = (const u16*)(ws + WS_KT) + (size_t)(head * 2 + hh) * S_ + r;
    const char* vfb = ws + WS_VF + (size_t)head * NTT * 1280 + hh * 320 + (r < 18 ? r : 18) * 16;

    bf16x8 qm[4]; bf16x4 qt[4];
#pragma unroll
    for (int i = 0; i < 4; ++i) {
        const int tq = qb * 8 + qg * 4 + i;
        qm[i] = *(const bf16x8*)(ws + WS_QM + ((size_t)(head * NTT + tq) * 64 + lane) * 16);
        qt[i] = (bf16x4){(short)((const u16*)(ws + WS_QT))[(size_t)(head * 2 + hh) * S_ + tq * 32 + r], 0, 0, 0};
    }
    f32x16 o[4] = {};
    const int kt0 = kh * 32;
    bf16x8 km = *(const bf16x8*)(kmb + (size_t)kt0 * 1024);
    short ktl = (short)ktb[kt0 * 32];
    bf16x8 v0 = *(const bf16x8*)(vfb + (size_t)kt0 * 1280), v1 = *(const bf16x8*)(vfb + (size_t)kt0 * 1280 + 640);
#pragma unroll 1
    for (int kt = kt0; kt < kt0 + 32; ++kt) {
        const int kn = min(kt + 1, NTT - 1);
        const bf16x8 km_n = *(const bf16x8*)(kmb + (size_t)kn * 1024);
        const short kt_n = (short)ktb[kn * 32];
        const bf16x8 v0_n = *(const bf16x8*)(vfb + (size_t)kn * 1280), v1_n = *(const bf16x8*)(vfb + (size_t)kn * 1280 + 640);
        const bf16x4 kt4 = {ktl, 0, 0, 0};
#pragma unroll
        for (int i = 0; i < 4; ++i) {
            f32x16 s = MFMA32(km, qm[i], (f32x16){});
            s = MFMA32K8(kt4, qt[i], s);
            u32x4 p0, p1;
#pragma unroll
            for (int e = 0; e < 4; ++e) {
                p0[e] = cvtpk(ex2(s[2 * e]), ex2(s[2 * e + 1]));
                p1[e] = cvtpk(ex2(s[8 + 2 * e]), ex2(s[8 + 2 * e + 1]));
            }
            o[i] = MFMA32(v0, __builtin_bit_cast(bf16x8, p0), o[i]);
            o[i] = MFMA32(v1, __builtin_bit_cast(bf16x8, p1), o[i]);
        }
        km = km_n; ktl = kt_n; v0 = v0_n; v1 = v1_n;
    }
    float* red = (float*)smem;
    int* flag = (int*)(smem + 4 * 2 * 11 * 256);
    if (tid == 0) *flag = 0;
#pragma unroll
    for (int t = 0; t < 2; ++t)
#pragma unroll
        for (int e = 0; e < 11; ++e) red[((w * 2 + t) * 11 + e) * 64 + lane] = kh ? o[t][e] : o[2 + t][e];
    __syncthreads();
    f32x16 fo[2];
    bool bad = false;
#pragma unroll
    for (int t = 0; t < 2; ++t) {
        fo[t] = kh ? o[2 + t] : o[t];
#pragma unroll
        for (int e = 0; e < 11; ++e) fo[t][e] += red[(((w ^ 2) * 2 + t) * 11 + e) * 64 + lane];
        const float l = __shfl(fo[t][10], r);
        bad |= !(l >= 0x1p-60f && l <= 0x1p60f);
#pragma unroll
        for (int e = 0; e < 8; ++e) bad |= !(fabsf(fo[t][e]) <= 0x1p120f);
        if (hh == 0) bad |= !(fabsf(fo[t][8]) <= 0x1p120f) || !(fabsf(fo[t][9]) <= 0x1p120f);
    }
    if (__any(bad) && lane == 0) *flag = 1;
    __syncthreads();
    if (*flag == 0) {
#pragma unroll
        for (int t = 0; t < 2; ++t) store_o(ws, head, qb * 8 + qg * 4 + 2 * kh + t, lane, fo[t]);
        return;
    }
#pragma unroll 1
    for (int t = 0; t < 2; ++t) {
        const int tq = qb * 8 + 2 * w + t;
        const bf16x8 fq = *(const bf16x8*)(ws + WS_QM + ((size_t)(head * NTT + tq) * 64 + lane) * 16);
        const bf16x4 fqt = {(short)((const u16*)(ws + WS_QT))[(size_t)(head * 2 + hh) * S_ + tq * 32 + r], 0, 0, 0};
        f32x16 acc = {};
        float m = -INFINITY;
#pragma unroll 1
        for (int kt = 0; kt < NTT; ++kt) {
            const bf16x8 fk = *(const bf16x8*)(kmb + (size_t)kt * 1024);
            const bf16x4 fkt = {(short)ktb[kt * 32], 0, 0, 0};
            const bf16x8 f0 = *(const bf16x8*)(vfb + (size_t)kt * 1280), f1 = *(const bf16x8*)(vfb + (size_t)kt * 1280 + 640);
            f32x16 s = MFMA32(fk, fq, (f32x16){});
            s = MFMA32K8(fkt, fqt, s);
            float mx = s[0];
#pragma unroll
            for (int e = 1; e < 16; ++e) mx = fmaxf(mx, s[e]);
            mx = fmaxf(mx, __shfl_xor(mx, 32));
            const float mn = fmaxf(m, mx);
            const float al = (mn == -INFINITY) ? 1.0f : ex2(m - mn);
            const float sub = (mn == -INFINITY) ? 0.0f : mn;
            m = mn;
            u32x4 p0, p1;
#pragma unroll
            for (int e = 0; e < 4; ++e) {
                p0[e] = cvtpk(ex2(s[2 * e] - sub), ex2(s[2 * e + 1] - sub));
                p1[e] = cvtpk(ex2(s[8 + 2 * e] - sub), ex2(s[8 + 2 * e + 1] - sub));
            }
#pragma unroll
            for (int e = 0; e < 16; ++e) acc[e] *= al;
            acc = MFMA32(f0, __builtin_bit_cast(bf16x8, p0), acc);
            acc = MFMA32(f1, __builtin_bit_cast(bf16x8, p1), acc);
        }
        store_o(ws, head, tq, lane, acc);
    }
}

constexpr int DN_SLOT = 64 * 320;
constexpr int K2_LDS = 3 * DN_SLOT;

template <int NT>
__device__ __forceinline__ void down_body(const u16* __restrict__ wd, char* __restrict__ ws, char* smem, int tp, int nt0, int sp) {
    const lds_char* lds = (const lds_char*)smem;
    const unsigned lds0 = (unsigned)(uintptr_t)lds;
    const int tid = threadIdx.x, lane = tid & 63, hh = lane >> 5;
    const int w = __builtin_amdgcn_readfirstlane(tid >> 6);
    const int tt = tp * 4 + w;
    unsigned voff[5];
#pragma unroll
    for (int i = 0; i < 5; ++i) {
        const int c = i * 64 + lane, row = c / 20;
        int cc = c - row * 20;
        if (cc >= NT * 4) cc = 0;
        voff[i] = (unsigned)((16 * w + row) * (D_ * 2) + cc * 16);
    }
    const char* wsrc = (const char*)(wd + (size_t)sp * 1152 * D_ + nt0 * 32);
    auto stage = [&](int s, int buf) {
#pragma unroll
        for (int i = 0; i < 5; ++i) glds16(wsrc + (size_t)s * 64 * (D_ * 2), voff[i], lds0 + buf * DN_SLOT + w * 5120 + i * 1024);
    };
    const char* hsrc = ws + WS_H + ((size_t)(tt * NKB + sp * 72) * 64) * 16;
    const unsigned hoff = lane * 16;
    auto hload = [&](bf16x8& dst, int kb) {
        asm volatile("global_load_dwordx4 %0, %1, %2" : "=v"(dst) : "v"(hoff), "s"(hsrc + (size_t)kb * 1024) : "memory");
    };
    const int grp = (lane >> 4) & 1, q4 = (lane & 15) >> 2, p4 = lane & 3;
    const lds_char* rp = lds + (8 * hh + q4) * 320 + grp * 32 + (((p4 & 1) << 4) | ((p4 >> 1) << 3));

    f32x16 acc[NT] = {};
    bf16x8 hA[4], hB[4];
    stage(0, 0);
#pragma unroll
    for (int i = 0; i < 4; ++i) hload(hA[i], i);
    stage(1, 1);
    auto compute = [&](const bf16x8 (&hf)[4], int buf) {
        const lds_char* p = rp + buf * DN_SLOT;
#pragma unroll
        for (int k = 0; k < 4; ++k)
#pragma unroll
            for (int t = 0; t < NT; ++t) {
                const bf16x8 wf = wfrag(p, k * 16 * 320 + t * 64, k * 16 * 320 + t * 64 + 4 * 320);
                acc[t] = MFMA32(wf, hf[k], acc[t]);
            }
    };
    int buf = 0;
#pragma unroll 1
    for (int s = 0; s < 18; s += 2) {
        asm volatile("s_waitcnt vmcnt(5)\n\ts_barrier" : "+v"(hA[0]), "+v"(hA[1]), "+v"(hA[2]), "+v"(hA[3]) :: "memory");
#pragma unroll
        for (int i = 0; i < 4; ++i) hload(hB[i], 4 * (s + 1) + i);
        { const int nb = buf == 0 ? 2 : buf - 1; stage(min(s + 2, 17), nb); }
        SBAR();
        compute(hA, buf);
        buf = buf == 2 ? 0 : buf + 1;
        asm volatile("s_waitcnt vmcnt(5)\n\ts_barrier" : "+v"(hB[0]), "+v"(hB[1]), "+v"(hB[2]), "+v"(hB[3]) :: "memory");
#pragma unroll
        for (int i = 0; i < 4; ++i) hload(hA[i], min(4 * (s + 2) + i, 71));
        { const int nb = buf == 0 ? 2 : buf - 1; stage(min(s + 3, 17), nb); }
        SBAR();
        compute(hB, buf);
        buf = buf == 2 ? 0 : buf + 1;
    }
    asm volatile("s_waitcnt vmcnt(0)" : "+v"(hA[0]), "+v"(hA[1]), "+v"(hA[2]), "+v"(hA[3]) :: "memory");
#pragma unroll
    for (int t = 0; t < NT; ++t) {
        char* pb = ws + WS_PD + ((size_t)((sp * NTT + tt) * 18 + nt0 + t) * 128 + lane) * 16;
        *(u32x4*)(pb) = (u32x4){cvtpk(acc[t][0], acc[t][1]), cvtpk(acc[t][2], acc[t][3]), cvtpk(acc[t][4], acc[t][5]), cvtpk(acc[t][6], acc[t][7])};
        *(u32x4*)(pb + 1024) = (u32x4){cvtpk(acc[t][8], acc[t][9]), cvtpk(acc[t][10], acc[t][11]), cvtpk(acc[t][12], acc[t][13]), cvtpk(acc[t][14], acc[t][15])};
    }
}

__global__ __launch_bounds__(256, 2) void k2_attn_down(const u16* __restrict__ wd, char* __restrict__ ws) {
    extern __shared__ __attribute__((aligned(16))) char smem[];
    const int bid = blockIdx.x;
    if (bid < 256) {
        attn_role(ws, smem, bid);
    } else {
        const int b = bid - 256, xcd = b & 7, idx = b >> 3;
        const int sp = xcd >> 1, tp = (xcd & 1) * 8 + (idx & 7), g = idx >> 3;
        if (g & 1) down_body<4>(wd, ws, smem, tp, g == 1 ? 5 : 14, sp);
        else down_body<5>(wd, ws, smem, tp, g == 0 ? 0 : 9, sp);
    }
}

__global__ __launch_bounds__(320) void k3_final(const u16* __restrict__ x, const char* __restrict__ ws, u16* __restrict__ out) {
    __shared__ __attribute__((aligned(16))) char of[NKS * 1024];
    const int tid = threadIdx.x, lane = tid & 63, r = lane & 31, hh = lane >> 5;
    const int w = __builtin_amdgcn_readfirstlane(tid >> 6);
    const int tt = blockIdx.x >> 2, g = blockIdx.x & 3;
    const int nt0 = g == 0 ? 0 : g == 1 ? 5 : g == 2 ? 9 : 14, ntn = (g & 1) ? 4 : 5;
    for (int i = tid; i < NKS * 64; i += 320) *(u32x4*)(of + i * 16) = *(const u32x4*)(ws + WS_OF + ((size_t)tt * NKS * 64 + i) * 16);
    const bool active = w < ntn;
    const int nt = nt0 + (active ? w : 0);
    bf16x8 wf[NKS];
#pragma unroll
    for (int ks = 0; ks < NKS; ++ks) wf[ks] = *(const bf16x8*)(ws + WS_WOF + ((size_t)(nt * NKS + ks) * 64 + lane) * 16);
    u32x4 pd[4][2];
#pragma unroll
    for (int sp = 0; sp < 4; ++sp) {
        const char* pb = ws + WS_PD + ((size_t)((sp * NTT + tt) * 18 + nt) * 128 + lane) * 16;
        pd[sp][0] = *(const u32x4*)pb; pd[sp][1] = *(const u32x4*)(pb + 1024);
    }
    const size_t xo = (size_t)(tt * 32 + r) * D_ + nt * 32 + 8 * hh;
    const u32x4 x0 = *(const u32x4*)(x + xo), x1 = *(const u32x4*)(x + xo + 16);
    __syncthreads();
    if (!active) return;
    f32x16 acc = {};
#pragma unroll
    for (int ks = 0; ks < NKS; ++ks) acc = MFMA32(wf[ks], *(const bf16x8*)(of + (ks * 64 + lane) * 16), acc);
    u32x4 res[2];
#pragma unroll
    for (int hf = 0; hf < 2; ++hf)
#pragma unroll
        for (int i = 0; i < 4; ++i) {
            float dlo = 0.f, dhi = 0.f;
#pragma unroll
            for (int sp = 0; sp < 4; ++sp) { dlo += bflo(pd[sp][hf][i]); dhi += bfhi(pd[sp][hf][i]); }
            const float glo = dlo * rcp(1.0f + ex2(-LOG2E * dlo)), ghi = dhi * rcp(1.0f + ex2(-LOG2E * dhi));
            const unsigned xv = hf ? x1[i] : x0[i];
            res[hf][i] = cvtpk(bflo(xv) + acc[hf * 8 + 2 * i] * glo, bfhi(xv) + acc[hf * 8 + 2 * i + 1] * ghi);
        }
    *(u32x4*)(out + xo) = res[0];
    *(u32x4*)(out + xo + 16) = res[1];
}

extern "C" void kernel_launch(void* const* d_in, const int* in_sizes, int n_in, void* d_out, int out_size, void* d_ws, size_t ws_size,
                              hipStream_t stream) {
    const u16* x = (const u16*)d_in[0];
    const u16* wqkv = (const u16*)d_in[1];
    const u16* wo = (const u16*)d_in[2];
    const u16* wg = (const u16*)d_in[4];
    const u16* wu = (const u16*)d_in[5];
    const u16* wd = (const u16*)d_in[6];
    char* ws = (char*)d_ws;
    (void)hipFuncSetAttribute((const void*)k1_proj, hipFuncAttributeMaxDynamicSharedMemorySize, K1_LDS);
    (void)hipFuncSetAttribute((const void*)k2_attn_down, hipFuncAttributeMaxDynamicSharedMemorySize, K2_LDS);
    k0_ln<<<256, 128, 0, stream>>>(x, wo, ws);
    k1_proj<<<256, 512, K1_LDS, stream>>>(wqkv, wg, wu, ws);
    k2_attn_down<<<512, 256, K2_LDS, stream>>>(wd, ws);
    k3_final<<<256, 320, 0, stream>>>(x, ws, (u16*)d_out);
}
```

```cpp
#include <hip/hip_runtime.h>
#include <hip/hip_bf16.h>
#include <stdint.h>

typedef __attribute__((ext_vector_type(8))) short bf16x8;
typedef __attribute__((ext_vector_type(4))) short bf16x4;
typedef __attribute__((ext_vector_type(16))) float f32x16;
typedef __attribute__((ext_vector_type(4))) float f32x4;
typedef __attribute__((ext_vector_type(4))) unsigned u32x4;
typedef __attribute__((ext_vector_type(2))) unsigned u32x2;
typedef __attribute__((address_space(3))) char lds_char;
typedef __attribute__((address_space(1))) unsigned gu32;
typedef unsigned short u16;

#define IMG_UNITS 2
#define G_PD 3
#define PF_DELAY 8
#define G_PRIO 0
#define ATTN_PAT 0
#define E1_POS 10
#define A_PRIO 0
#define G_FLIP 0
#define G_PD2 3
#define G_THR 0
#define PIN(x) asm volatile("" : "+v"(x))
#define STAMP(k, i)
constexpr int S_ = 2048, D_ = 576, NH = 32, DH = 18, F_ = 4608;
constexpr int NTT = S_ / 32;
constexpr int NKS = D_ / 16;
constexpr int NKB = F_ / 16;
constexpr float QSCALE = 0.23570226039551584f * 1.4426950408889634f;
constexpr float LOG2E = 1.4426950408889634f;

constexpr size_t WS_XN  = 0;
constexpr size_t WS_WQ  = WS_XN + (size_t)NTT * NKS * 1024;
constexpr size_t WS_WG  = WS_WQ + (size_t)NH * 73728;
constexpr size_t WS_WOF = WS_WG + (size_t)32 * IMG_UNITS * 73728;
constexpr size_t WS_QM  = WS_WOF + (size_t)18 * NKS * 1024;
constexpr size_t WS_QT  = WS_QM + (size_t)NH * NTT * 1024;
constexpr int KV_REC = 2560, KV_V = 1024, KV_T = 2304;
constexpr size_t WS_KV  = WS_QT + (size_t)NH * 2 * S_ * 4;
constexpr size_t WS_H   = WS_KV + (size_t)NH * NTT * KV_REC;
constexpr size_t WS_OF  = WS_H + (size_t)NTT * NKB * 1024;
constexpr size_t WS_OT  = WS_OF + (size_t)NTT * NKS * 1024;
constexpr size_t WS_PD  = WS_OT + (size_t)NTT * NH * 128;
constexpr size_t WS_CNT = WS_PD + (size_t)4 * NTT * 18 * 2048;
constexpr int CNT_QKV = 0;
constexpr int CNT_H = 32;
constexpr int CNT_O = 64;
constexpr int CNT_P = 72;
constexpr int CNT_N = 136;
constexpr size_t WS_END = WS_CNT + (size_t)CNT_N * 128;
static_assert(WS_END < (1ull << 32), "workspace offsets are 32-bit");

#define MFMA32(a, b, c) __builtin_amdgcn_mfma_f32_32x32x16_bf16(a, b, c, 0, 0, 0)
#define MFMA32K8(a, b, c) __builtin_amdgcn_mfma_f32_32x32x8bf16_1k(a, b, c, 0, 0, 0)
#define SBAR() __builtin_amdgcn_sched_barrier(0)

__device__ __forceinline__ unsigned cvtpk(float lo, float hi) {
    typedef __attribute__((ext_vector_type(2))) float f2_t;
    typedef __attribute__((ext_vector_type(2))) __bf16 b2_t;
    return __builtin_bit_cast(unsigned, __builtin_convertvector((f2_t){lo, hi}, b2_t));
}
template <int V> struct IC { static constexpr int value = V; };
template <int N, int E, class F>
__device__ __forceinline__ void static_for(F&& f) {
    if constexpr (N < E) { f(IC<N>{}); static_for<N + 1, E>(f); }
}
__device__ __forceinline__ float bf2f(u16 v) { return __uint_as_float((unsigned)v << 16); }
__device__ __forceinline__ float bflo(unsigned v) { return __uint_as_float(v << 16); }
__device__ __forceinline__ float bfhi(unsigned v) { return __uint_as_float(v & 0xffff0000u); }
__device__ __forceinline__ float rbf(float v) { return bflo(cvtpk(v, v)); }
__device__ __forceinline__ float ex2(float v) { return __builtin_amdgcn_exp2f(v); }
__device__ __forceinline__ float rcp(float v) { return __builtin_amdgcn_rcpf(v); }

#define GLDS_(n) "global_load_lds_dwordx4 %0, %1 offset:" #n "\n\t"
template <int N>
__device__ __forceinline__ void glds16n(const void* sbase, unsigned voff, unsigned lds_base) {
    static_assert(N >= 1 && N <= 4, "");
    if constexpr (N == 1) asm volatile("s_mov_b32 m0, %2\n\ts_nop 0\n\t" GLDS_(0) :: "v"(voff), "s"(sbase), "s"(lds_base) : "memory");
    if constexpr (N == 2) asm volatile("s_mov_b32 m0, %2\n\ts_nop 0\n\t" GLDS_(0) GLDS_(1024) :: "v"(voff), "s"(sbase), "s"(lds_base) : "memory");
    if constexpr (N == 3) asm volatile("s_mov_b32 m0, %2\n\ts_nop 0\n\t" GLDS_(0) GLDS_(1024) GLDS_(2048) :: "v"(voff), "s"(sbase), "s"(lds_base) : "memory");
    if constexpr (N == 4) asm volatile("s_mov_b32 m0, %2\n\ts_nop 0\n\t" GLDS_(0) GLDS_(1024) GLDS_(2048) GLDS_(3072) :: "v"(voff), "s"(sbase), "s"(lds_base) : "memory");
}
__device__ __forceinline__ void glds16(const void* sbase, unsigned voff, unsigned lds_base) { glds16n<1>(sbase, voff, lds_base); }
typedef short v4i16_t __attribute__((ext_vector_type(4)));
__device__ __forceinline__ bf16x4 trread(const lds_char* p) {
    return __builtin_bit_cast(bf16x4, __builtin_amdgcn_ds_read_tr16_b64_v4i16((__attribute__((address_space(3))) v4i16_t*)p));
}
__device__ __forceinline__ bf16x8 wfrag(const lds_char* p, int off0, int off1) {
    bf16x4 a = trread(p + off0), b = trread(p + off1);
    return (bf16x8){a[0], a[1], a[2], a[3], b[0], b[1], b[2], b[3]};
}
__device__ __forceinline__ int colperm(int rho) { const int p = rho >> 2; return (((p & 1) << 3) | ((p >> 1) << 2)) + (rho & 3); }

__device__ __forceinline__ const lds_char* opaque_lds(unsigned a) { asm volatile("" : "+v"(a)); return (const lds_char*)(uintptr_t)a; }
__device__ __forceinline__ int fresh_tid() { int t = threadIdx.x; asm volatile("" : "+v"(t)); return t; }

__device__ __forceinline__ void st16_dev(const void* sbase, unsigned voff, u32x4 v) {
    asm volatile("global_store_dwordx4 %0, %1, %2 sc1\n\ts_nop 1" :: "v"(voff), "v"(v), "s"(sbase) : "memory");
}
__device__ __forceinline__ gu32* cnt_ptr(const char* ws, int cnt) { return (gu32*)(ws + WS_CNT + (size_t)cnt * 128); }
__device__ __forceinline__ void cnt_add(const char* ws, int cnt) {
    if (threadIdx.x == 0) asm volatile("global_atomic_add %0, %1, %2" :: "v"(0u), "v"(1u), "s"(ws + WS_CNT + (size_t)cnt * 128) : "memory");
}
__device__ __forceinline__ void cnt_peek(const char* ws, int cnt, unsigned& v) {
    asm volatile("global_load_dword %0, %1, %2 sc1" : "=v"(v) : "v"(0u), "s"(ws + WS_CNT + (size_t)cnt * 128) : "memory");
}
__device__ __forceinline__ void cnt_wait(const char* ws, int cnt, unsigned v, unsigned n) {
    unsigned sv, it;
    asm volatile("s_mov_b32 %2, 0x10000\n"
                 "1:\n\t"
                 "s_nop 0\n\t"
                 "v_readfirstlane_b32 %1, %0\n\t"
                 "s_nop 3\n\t"
                 "s_cmp_ge_u32 %1, %3\n\t"
                 "s_cbranch_scc1 2f\n\t"
                 "s_sub_u32 %2, %2, 1\n\t"
                 "s_cbranch_scc1 2f\n\t"
                 "s_sleep 1\n\t"
                 "global_load_dword %0, %4, %5 sc1\n\t"
                 "s_waitcnt vmcnt(0)\n\t"
                 "s_branch 1b\n"
                 "2:"
                 : "+v"(v), "=&s"(sv), "=&s"(it) : "s"(n), "v"(0u), "s"(ws + WS_CNT + (size_t)cnt * 128) : "scc", "memory");
}
__device__ __forceinline__ void acquire_issue() { asm volatile("buffer_inv sc1" ::: "memory"); }

constexpr int K0_THREADS = 1024;
template <int CTRL>
__device__ __forceinline__ float dpp_add(float v) {
    return v + __uint_as_float(__builtin_amdgcn_update_dpp(0u, __float_as_uint(v), CTRL, 0xf, 0xf, true));
}
__device__ __forceinline__ float sum32(float v) {
    v = dpp_add<0xB1>(v);
    v = dpp_add<0x4E>(v);
    v = dpp_add<0x141>(v);
    v = dpp_add<0x140>(v);
    const auto sw = __builtin_amdgcn_permlane16_swap(__float_as_uint(v), __float_as_uint(v), false, false);
    return __uint_as_float(sw[0]) + __uint_as_float(sw[1]);
}
__global__ __launch_bounds__(K0_THREADS) void k0_prep(const u16* __restrict__ x, const u16* __restrict__ wqkv, const u16* __restrict__ wo,
                                                      const u16* __restrict__ wg, const u16* __restrict__ wu, char* __restrict__ ws) {
    __shared__ __attribute__((aligned(16))) char lds_k0[8 * 1024];
    char* sink = lds_k0;
    char* wol = lds_k0 + 4096;
    const int lane = threadIdx.x & 63;
    const int w = __builtin_amdgcn_readfirstlane(threadIdx.x >> 6);
    const int xcd = blockIdx.x & 7, idx = blockIdx.x >> 3;
    STAMP(0, 0);
    if (blockIdx.x == 0 && threadIdx.x < CNT_N) *(unsigned*)(ws + WS_CNT + (size_t)threadIdx.x * 128) = 0u;
    if (w < 4) {
        const int row = blockIdx.x * 8 + w * 2 + (lane >> 5), c = lane & 31;
        const bool third = c < 8;
        u32x4 v[3];
        v[0] = *(const u32x4*)(x + (size_t)row * D_ + c * 8);
        v[1] = *(const u32x4*)(x + (size_t)row * D_ + (c + 32) * 8);
        v[2] = *(const u32x4*)(x + (size_t)row * D_ + (third ? c + 64 : c) * 8);
        float s = 0.f, s3 = 0.f;
#pragma unroll
        for (int j = 0; j < 4; ++j) { s += (bflo(v[0][j]) + bfhi(v[0][j])) + (bflo(v[1][j]) + bfhi(v[1][j])); s3 += bflo(v[2][j]) + bfhi(v[2][j]); }
        const float mean = sum32(third ? s + s3 : s) * (1.0f / D_);
        float q = 0.f, q3 = 0.f;
#pragma unroll
        for (int j = 0; j < 4; ++j) {
            const float a0 = bflo(v[0][j]) - mean, b0 = bfhi(v[0][j]) - mean, a1 = bflo(v[1][j]) - mean, b1 = bfhi(v[1][j]) - mean;
            const float a2 = bflo(v[2][j]) - mean, b2 = bfhi(v[2][j]) - mean;
            q += a0 * a0 + b0 * b0 + a1 * a1 + b1 * b1; q3 += a2 * a2 + b2 * b2;
        }
        const float vq = sum32(third ? q + q3 : q);
        const float mb = rbf(mean), var = rbf(vq * (1.0f / D_));
        const float isd = 1.0f / rbf(sqrtf(rbf(var + 1e-5f)));
        const int tt = row >> 5, r = row & 31;
        char* dst = ws + WS_XN;
#pragma unroll
        for (int i = 0; i < 3; ++i) {
            u32x4 o;
#pragma unroll
            for (int j = 0; j < 4; ++j) {
                const unsigned d = cvtpk(bflo(v[i][j]) - mb, bfhi(v[i][j]) - mb);
                o[j] = cvtpk(bflo(d) * isd, bfhi(d) * isd);
            }
            const int qd = c + 32 * i, ks = qd >> 1, hh = qd & 1;
            if (i < 2 || third) *(u32x4*)(dst + ((size_t)(tt * NKS + ks) * 64 + hh * 32 + r) * 16) = o;
        }
    } else if (w < 6) {
        typedef u32x4 u32x4_a4 __attribute__((aligned(4)));
        const int t = (w - 4) * 64 + lane;
        const int head = blockIdx.x >> 3;
        const int row0 = (blockIdx.x & 7) * 72;
        u32x4 val[5]; unsigned kt[5];
#pragma unroll
        for (int i = 0; i < 5; ++i) {
            const int cidx = min(t + 128 * i, 575), row = row0 + (cidx >> 3), ch = cidx & 7;
            const u16* src = wqkv + (size_t)row * (3 * D_) + head * DH;
            const int o0 = ch < 2 ? 8 * ch : ch < 4 ? 2 * D_ + 8 * (ch - 2) : ch == 4 ? 2 * D_ + 10 : ch == 5 ? 10 : D_ + 8 * (ch - 6);
            val[i] = *(const u32x4_a4*)(src + o0);
            kt[i] = *(const unsigned*)(src + D_ + 16);
        }
#pragma unroll
        for (int i = 0; i < 5; ++i) {
            const int cidx = t + 128 * i;
            if (cidx < 576) {
                const int row = row0 + (cidx >> 3), ch = cidx & 7;
                u32x4 o = val[i];
                if (ch == 4) o = (u32x4){val[i][3], 0u, 0u, 0u};
                if (ch == 5) o = (u32x4){val[i][3], kt[i], 0u, 0u};
                *(u32x4*)(ws + WS_WQ + (size_t)head * 73728 + (row >> 6) * 8192 + (ch >> 2) * 4096 + (row & 63) * 64 + (ch & 3) * 16) = o;
            }
        }
    } else if (w < 8) {
        const int f0 = blockIdx.x + 256 * (w - 6);
        const unsigned dst = (unsigned)(uintptr_t)(lds_char*)wol + (w - 6) * 2048;
#pragma unroll
        for (int q = 0; q < 2; ++q) {
            const int f = min(f0 + 512 * q, 18 * NKS - 1), nt = f / NKS, ks = f - nt * NKS;
            const int k = lane >> 2;
            const int krow = ks < 32 ? ks * DH + k : (8 * (ks - 32) + (k >> 1)) * DH + 16 + (k & 1);
            glds16(wo + nt * 32, (unsigned)(krow * (D_ * 2) + (lane & 3) * 16), dst + q * 1024);
        }
        asm volatile("s_waitcnt vmcnt(0)" ::: "memory");
        const int hh = lane >> 5, grp = (lane >> 4) & 1, q4 = (lane & 15) >> 2, p4 = lane & 3;
        const lds_char* p = (const lds_char*)wol + (w - 6) * 2048 + (8 * hh + q4) * 64 + grp * 32 + (((p4 & 1) << 4) | ((p4 >> 1) << 3));
#pragma unroll
        for (int q = 0; q < 2; ++q) {
            const int f = f0 + 512 * q;
            const bf16x8 v = wfrag(p, q * 1024, q * 1024 + 256);
            if (f < 18 * NKS) *(bf16x8*)(ws + WS_WOF + ((size_t)f * 64 + lane) * 16) = v;
        }
    } else if (IMG_UNITS == 5) {
        const int t = threadIdx.x - 512;
        const int mat = blockIdx.x >> 7, rb = (blockIdx.x >> 2) & 31, cq = blockIdx.x & 3;
        const u16* src = mat ? wu : wg;
        u32x4 val[6]; unsigned dof[6];
#pragma unroll
        for (int i = 0; i < 6; ++i) {
            const int cidx = min(i * 512 + t, 2591);
            const int pr = cidx / 288, e = cidx - pr * 288, g8 = e / 36, e36 = e - g8 * 36;
            const int u = e36 < 32 ? e36 >> 3 : 4;
            const int par = e36 < 32 ? (e36 >> 2) & 1 : (e36 - 32) >> 1;
            const int c4 = e36 < 32 ? e36 & 3 : (e36 - 32) & 1;
            const int row = rb * 18 + 2 * pr + par, grp = cq * 8 + g8;
            val[i] = *(const u32x4*)(src + (size_t)row * F_ + grp * 144 + u * 32 + c4 * 8);
            dof[i] = (unsigned)((grp * 5 + u) * 73728 + (row >> 6) * 8192 + mat * 4096 + (row & 63) * 64 + ((c4 ^ (mat << 1)) << 4));
        }
#pragma unroll
        for (int i = 0; i < 6; ++i)
            if (i * 512 + t < 2592) *(u32x4*)(ws + WS_WG + dof[i]) = val[i];
    } else if (w < 12) {
        const int t = threadIdx.x - 512;
        const int mat = blockIdx.x >> 7, rb = (blockIdx.x >> 2) & 31, cq = blockIdx.x & 3;
        const u16* src = mat ? wu : wg;
        u32x4 val[5]; unsigned dof[5];
#pragma unroll
        for (int i = 0; i < 5; ++i) {
            const int cidx = min(i * 256 + t, 1151);
            const int pr = cidx >> 7, g8 = (cidx >> 4) & 7, u = (cidx >> 3) & 1, par = (cidx >> 2) & 1, c4 = cidx & 3;
            const int row = rb * 18 + 2 * pr + par, grp = cq * 8 + g8;
            val[i] = *(const u32x4*)(src + (size_t)row * F_ + grp * 144 + u * 32 + c4 * 8);
            dof[i] = (unsigned)((grp * IMG_UNITS + u) * 73728 + (row >> 6) * 8192 + mat * 4096 + (row & 63) * 64 + ((c4 ^ (mat << 1)) << 4));
        }
#pragma unroll
        for (int i = 0; i < 5; ++i)
            if (i * 256 + t < 1152) *(u32x4*)(ws + WS_WG + dof[i]) = val[i];
    } else {
        const int t = threadIdx.x - 768;
        const int mat = blockIdx.x >> 7, rb = (blockIdx.x >> 2) & 31, cq = blockIdx.x & 3;
        const char* src = (const char*)(mat ? wu : wg) + (size_t)rb * 18 * (F_ * 2) + cq * 2304;
        const unsigned dst = (unsigned)(uintptr_t)(lds_char*)sink + (w - 12) * 1024;
        __builtin_amdgcn_s_sleep(PF_DELAY);
#pragma unroll
        for (int i = 0; i < 2; ++i) {
            const int cidx = min(i * 256 + t, 287), row = cidx >> 4, g8 = (cidx >> 1) & 7, e = cidx & 1;
            glds16(src, (unsigned)(row * (F_ * 2) + g8 * 288 + 128 + e * 48), dst);
        }
        asm volatile("s_waitcnt vmcnt(0)" ::: "memory");
    }
    STAMP(0, 1);
}

constexpr int K1_UNIT = 73728, K1_LDS = 163840;
constexpr int WD = 3;
constexpr int k1_slot(int half, int s) { return half ? K1_UNIT + 8192 * s : (s < 7 ? 8192 * s : 2 * K1_UNIT + 8192 * (s - 7)); }

__device__ __forceinline__ void dma16_piece(const void* sbase, unsigned voff, unsigned add, unsigned lds_base) {
    unsigned vt;
    asm volatile("s_mov_b32 m0, %3\n\tv_add_u32 %0, %4, %1\n\tglobal_load_lds_dwordx4 %0, %2"
                 : "=&v"(vt) : "v"(voff), "s"(sbase), "s"(lds_base), "i"(add) : "memory");
}

constexpr int AT_STAGE = 4 * KV_REC;
constexpr int DN_SLAB = 144 * 320;
constexpr int DN_RING = 4 * AT_STAGE;
constexpr int AT_RED = DN_RING + 2 * DN_SLAB;
constexpr int AT_Q = 2 * K1_UNIT;
constexpr int FN_O = 0, FN_OWN = DN_RING;
__device__ __forceinline__ int attn_head(int bid) { return ((bid & 7) >> 1) * 8 + (bid & 1) * 4 + ((bid >> 3) & 3); }

struct PCtx {
    const char* gu_src;
    const char* img_src;
    const char* ws;
    char* hdst;
    unsigned voff, sdst;
    unsigned rb0, rb1, lo;
    const char *wg_grp, *wu_grp;
    int w, hook_cnt, panel;
    unsigned lds0;
};
__device__ __forceinline__ void attn_prestage(const PCtx& c, int i) {
    const int head = attn_head(blockIdx.x), qb = blockIdx.x >> 5;
    if (i < 3) {
        const int id = c.w + 8 * i;
        if (id < 20) {
            const int kh = id >= 10, p = id - 10 * kh;
            glds16n<1>(c.ws + WS_KV + ((size_t)head * NTT + kh * 32) * KV_REC + p * 1024, c.lo, c.lds0 + kh * AT_STAGE + p * 1024);
        }
    }
    if (i == 3) glds16n<1>(c.ws + WS_QM + (size_t)(head * NTT + qb * 8 + c.w) * 1024, c.lo, c.lds0 + AT_Q + c.w * 1024);
    if (i == 4 && c.w < 2) {
        const unsigned lane = c.lo >> 4;
        glds16n<1>(c.ws + WS_QT + ((size_t)head * 2 * S_ + (qb * 8 + c.w * 4) * 32) * 4, (lane >> 5) * (S_ * 4) + (lane & 31) * 16, c.lds0 + AT_Q + 8192 + c.w * 1024);
    }
}

template <int HALF, int TL, bool EPI, int STG, int NEXT, int HOOK>
__device__ __forceinline__ void p_tile(const PCtx& c, const bf16x8 (&xf)[NKS], bf16x8 (&wb)[WD + 1],
                                       f32x16& acc, const f32x16& prev, int kb_prev, int stg_unit, unsigned& peek) {
    float t0 = 0.f, t1 = 0.f; u32x4 hv;
    const unsigned rb = TL ? c.rb1 : c.rb0;
    const lds_char* p = opaque_lds(rb + k1_slot(HALF, 0));
    const char* stg_src = c.gu_src + 64 * stg_unit;
    unsigned stg_vo = c.voff;
    if (STG == 2) stg_vo &= ~32u;
#pragma unroll
    for (int ks = 0; ks < NKS; ++ks) {
        const int kn = ks + WD;
        if (kn == 20) p = opaque_lds(rb + k1_slot(HALF, 5));
        if (kn == 28) p = opaque_lds(rb + k1_slot(HALF, 7));
        if (HOOK == 4 && ks == 0) cnt_peek(c.ws, c.hook_cnt, peek);
        if (HOOK == 2 && ks == 2) {
            asm volatile("s_waitcnt vmcnt(2)" : "+v"(peek) :: "memory");
            if (c.w == 0) { cnt_wait(c.ws, c.hook_cnt, peek, 8); acquire_issue(); }
        }
        if (HOOK == 3 && ks >= 1 && ks <= 9 && (ks & 1) == 1) attn_prestage(c, (ks - 1) >> 1);
        if (HOOK == 5 && ks == 28) {
            const int e = c.w * 64 + (int)(c.lo >> 4);
            const int ee = min(e, 287), row = c.panel * 72 + (ee >> 2), mat = (ee >> 1) & 1;
            const char* base = mat ? c.wu_grp : c.wg_grp;
            if (c.w < 5) {
                const unsigned long long a = (unsigned long long)base + (size_t)row * (F_ * 2) + 192 + (ee & 1) * 80;
                asm volatile("s_mov_b32 m0, %1\n\ts_nop 0\n\tglobal_load_lds_dwordx4 %0, off" :: "v"(a), "s"(c.lds0 + 57344 + c.w * 1024) : "memory");
            }
        }
        if (kn == NKS && NEXT == 2) {
            if (HOOK == 4) asm volatile("s_waitcnt vmcnt(2) lgkmcnt(0)" ::: "memory");
            else asm volatile("s_waitcnt vmcnt(1) lgkmcnt(0)" ::: "memory");
            asm volatile("s_barrier" ::: "memory");
            if (HOOK == 1) cnt_add(c.ws, c.hook_cnt);
        }
        if (kn == NKS && NEXT == 1) p = opaque_lds(c.rb1 + k1_slot(HALF, 0));
        if (kn == NKS && NEXT == 2) p = opaque_lds(c.rb0 + k1_slot(HALF ^ 1, 0));
        if (kn < NKS) {
            const int sl = kn >> 2, s0 = sl < 5 ? 0 : sl < 7 ? 5 : 7;
            const int off = (sl - s0) * 8192 + (kn & 3) * 1024;
            wb[kn % (WD + 1)] = wfrag(p, off, off + 256);
        } else if (NEXT != 0) {
            const int off = (kn - NKS) * 1024;
            wb[kn % (WD + 1)] = wfrag(p, off, off + 256);
        }
        constexpr int S0 = HOOK == 5 ? 9 : 1;
        if (STG != 0 && ks >= S0 && ks < S0 + 18 && (ks - S0) % 2 == 0) {
            const int s = (ks - S0) / 2;
            if (IMG_UNITS == 5) glds16(c.img_src + (size_t)stg_unit * 73728 + s * 8192, c.lo, c.lds0 + c.w * 1024 + k1_slot(HALF ^ 1, s));
            else dma16_piece(stg_src, stg_vo, (unsigned)(s * 64 * F_ * 2), c.sdst + k1_slot(HALF ^ 1, s));
        }
        const bf16x8 w = wb[ks % (WD + 1)];
        if (ks == 0) acc = MFMA32(w, xf[0], (f32x16){});
        else acc = MFMA32(w, xf[ks], acc);
        if (EPI && ks >= 2 && ks < 34) {
            const int e = (ks - 2) >> 2, ph = (ks - 2) & 3;
            float& t = (e & 1) ? t1 : t0;
            if (ph == 0) { t = ex2(-LOG2E * prev[e]); PIN(t); }
            if (ph == 1) { t = rcp(1.0f + t); PIN(t); }
            if (ph == 2) { t = t * prev[8 + e]; PIN(t); }
            if (ph == 3 && (e & 1)) { unsigned pk = cvtpk(t0, t1); PIN(pk); hv[e >> 1] = pk; }
        }
        if (EPI && ks == 34) st16_dev(c.hdst + (size_t)kb_prev * 1024, c.lo, hv);
        SBAR();
    }
}
__device__ __forceinline__ void glu_store(const PCtx& c, const f32x16& a, int kb) {
    float hv[8];
#pragma unroll
    for (int i = 0; i < 8; ++i) hv[i] = a[8 + i] * rcp(1.0f + ex2(-LOG2E * a[i]));
    st16_dev(c.hdst + (size_t)kb * 1024, c.lo, (u32x4){cvtpk(hv[0], hv[1]), cvtpk(hv[2], hv[3]), cvtpk(hv[4], hv[5]), cvtpk(hv[6], hv[7])});
}

__device__ __forceinline__ void phase_proj(const u16* __restrict__ wg, const u16* __restrict__ wu, char* __restrict__ ws, unsigned lds0) {
    const int tid = fresh_tid(), lane = tid & 63, r = lane & 31, hh = lane >> 5;
    const int w = __builtin_amdgcn_readfirstlane(tid >> 6);
    const int xcd = blockIdx.x & 7, idx = blockIdx.x >> 3;
    const int panel = (xcd & 1) * 4 + (idx & 3), j = (xcd >> 1) * 8 + (idx >> 2);
    const int tt = panel * 8 + w;

    PCtx c;
    {
        const int srow = lane >> 2, scp = lane & 3;
        const int schunk = (w < 4) ? scp : (scp ^ 2);
        c.gu_src = (const char*)((w < 4 ? wg : wu) + 144 * j);
        c.img_src = ws + WS_WG + (size_t)j * IMG_UNITS * 73728 + w * 1024;
        c.wg_grp = (const char*)(wg + 144 * j); c.wu_grp = (const char*)(wu + 144 * j); c.panel = panel;
        c.ws = ws; c.w = w; c.hook_cnt = CNT_QKV + j; c.lds0 = lds0;
        c.voff = (unsigned)((16 * (w & 3) + srow) * (F_ * 2) + schunk * 16);
        c.sdst = lds0 + (w >> 2) * 4096 + (w & 3) * 1024;
        c.hdst = ws + WS_H + (size_t)(tt * NKB + 9 * j) * 1024;
        c.lo = lane * 16;
        const int grp = (lane >> 4) & 1, q4 = (lane & 15) >> 2, p4 = lane & 3;
        const unsigned rd = lds0 + grp * 4096 + (8 * hh + q4) * 64 + (((p4 & 1) << 4) | ((p4 >> 1) << 3));
        c.rb0 = rd + 32 * (0 ^ grp);
        c.rb1 = rd + 32 * (1 ^ grp);
    }

    bf16x8 xf[NKS];
    f32x16 aQ = {}, aV = {}, aC = {}, aD = {};
    {
        const char* xsrc = ws + WS_XN + (size_t)tt * NKS * 1024;
        const char* qsrc = ws + WS_WQ + (size_t)j * 73728 + w * 1024;
        const char* isrc = c.img_src;
        auto GI = [&](auto sc_, auto ic_) __attribute__((always_inline)) {
            constexpr int s = decltype(sc_)::value, i = decltype(ic_)::value;
            if constexpr (i < 4) asm volatile("global_load_dwordx4 %0, %1, %2" : "=&v"(xf[4 * s + i]) : "v"(c.lo), "s"(xsrc + (4 * s + i) * 1024) : "memory");
            else if constexpr (i == 4) glds16(qsrc + s * 8192, c.lo, lds0 + w * 1024 + s * 8192);
            else glds16(isrc + s * 8192, c.lo, lds0 + w * 1024 + k1_slot(1, s));
        };
        auto U1 = [&](auto sc_) __attribute__((always_inline)) {
            constexpr int s = decltype(sc_)::value;
            glds16(isrc + 73728 + s * 8192, c.lo, lds0 + w * 1024 + k1_slot(0, s));
        };
        static_for<0, 6>([&](auto i) __attribute__((always_inline)) { GI(IC<0>{}, i); });
        static_for<0, 6>([&](auto i) __attribute__((always_inline)) { GI(IC<1>{}, i); });
        constexpr int PD = 4;
        bf16x8 fr[PD + 1];
        u32x4 oq, pk0, pk1;
        const char* rec = ws + WS_KV + (size_t)(j * NTT + tt) * KV_REC;
        const lds_char *pa0, *pa1, *pb0, *pb1;
        auto bases = [&](int s) __attribute__((always_inline)) {
            pa0 = opaque_lds(c.rb0 + 8192 * s); pa1 = opaque_lds(c.rb1 + 8192 * s);
            pb0 = opaque_lds(c.rb0 + K1_UNIT + 8192 * s); pb1 = opaque_lds(c.rb1 + K1_UNIT + 8192 * s);
        };
#define P1_K(n) ((n) < 128 ? (((n) >> 2) & 3) : ((((n) - 128) >> 1) & 3))
#define P1_T(n) ((n) < 128 ? ((n) & 3) : ((((n) - 128) >> 3) * 2 + ((n) & 1)))
        auto rdfrag = [&](auto nc) __attribute__((always_inline)) {
            constexpr int n = decltype(nc)::value, k = P1_K(n), t = P1_T(n);
            const lds_char* p = t == 0 ? pa0 : t == 1 ? pa1 : t == 2 ? pb0 : pb1;
            fr[n % (PD + 1)] = wfrag(p, k * 1024, k * 1024 + 256);
        };
#define WAITX(s, n) asm volatile("s_waitcnt vmcnt(" #n ")\n\ts_barrier" : "+v"(xf[4 * (s)]), "+v"(xf[4 * (s) + 1]), "+v"(xf[4 * (s) + 2]), "+v"(xf[4 * (s) + 3]) :: "memory")
        WAITX(0, 6);
        STAMP(1, 1);
        bases(0);
        static_for<0, PD>(rdfrag);
        static_for<0, 4 * NKS>([&](auto nc) __attribute__((always_inline)) {
            constexpr int n = decltype(nc)::value, m = n + PD;
            if constexpr (m < 4 * NKS && (m & 15) == 0) {
                constexpr int s1 = m >> 4;
                if constexpr (s1 < 8) { WAITX(s1, 7); } else { WAITX(s1, 1); }
                bases(s1);
            }
            if constexpr (m < 4 * NKS) rdfrag(IC<m>{});
            constexpr int per = (n + 4) >> 4, pos = per == 0 ? n : ((n + 4) & 15);
            constexpr int item = per == 0 ? (pos < 7 ? pos : -1) : ((pos & 1) == 0 && pos < 14 ? pos >> 1 : -1);
            if constexpr (item == 0 && per < 9) U1(IC<(per == 0 ? 7 : per == 1 ? 8 : per - 2)>{});
            if constexpr (item > 0 && per + 2 < 9) GI(IC<per + 2>{}, IC<item - 1>{});
            constexpr int ks = 4 * (n >> 4) + P1_K(n), t = P1_T(n);
            const bf16x8 f = fr[n % (PD + 1)];
            if constexpr (t == 0) { aQ = MFMA32(f, xf[ks], aQ); PIN(aQ); }
            if constexpr (t == 1) { aV = MFMA32(xf[ks], f, aV); PIN(aV); }
            if constexpr (t == 2) { aC = MFMA32(f, xf[ks], aC); PIN(aC); }
            if constexpr (t == 3) { aD = MFMA32(f, xf[ks], aD); PIN(aD); }
            if constexpr (n == 137) {
                oq = (u32x4){cvtpk(aQ[0] * QSCALE, aQ[1] * QSCALE), cvtpk(aQ[2] * QSCALE, aQ[3] * QSCALE),
                             cvtpk(aQ[4] * QSCALE, aQ[5] * QSCALE), cvtpk(aQ[6] * QSCALE, aQ[7] * QSCALE)};
                st16_dev(ws + WS_QM + (size_t)(j * NTT + tt) * 1024, c.lo, oq);
            }
            if constexpr (n == 138) {
                oq = (u32x4){cvtpk(aQ[8], aQ[9]), cvtpk(aQ[10], aQ[11]), cvtpk(aQ[12], aQ[13]), cvtpk(aQ[14], aQ[15])};
                st16_dev(rec, c.lo, oq);
            }
            if constexpr (n == 139) {
                const float sc = (r == 20 || r == 21) ? QSCALE : 1.0f;
#pragma unroll
                for (int i = 0; i < 4; ++i) pk0[i] = (r == 18) ? 0x3F803F80u : (r == 19) ? 0u : cvtpk(aV[2 * i] * sc, aV[2 * i + 1] * sc);
                PIN(pk0);
            }
            if constexpr (n == 140) {
                const float sc = (r == 20 || r == 21) ? QSCALE : 1.0f;
#pragma unroll
                for (int i = 0; i < 4; ++i) pk1[i] = (r == 18) ? 0x3F803F80u : (r == 19) ? 0u : cvtpk(aV[8 + 2 * i] * sc, aV[8 + 2 * i + 1] * sc);
                PIN(pk1);
            }
            if constexpr (n == 141) {
                if (r <= 19) {
                    st16_dev(rec + KV_V, hh * 320 + r * 16, pk0);
                    st16_dev(rec + KV_V + 640, hh * 320 + r * 16, pk1);
                }
            }
            if constexpr (n == 142) {
                if (r >= 20 && r < 24) {
                    const unsigned tb = r < 22 ? (unsigned)(WS_QT + ((size_t)(j * 2 + (r & 1)) * S_ + tt * 32 + 4 * hh) * 4)
                                               : (unsigned)(WS_KV + (size_t)(j * NTT + tt) * KV_REC + KV_T + (r & 1) * 128 + hh * 16);
                    st16_dev(ws, tb,      (u32x4){pk0[0] & 0xffffu, pk0[0] >> 16, pk0[1] & 0xffffu, pk0[1] >> 16});
                    st16_dev(ws, tb + 32, (u32x4){pk0[2] & 0xffffu, pk0[2] >> 16, pk0[3] & 0xffffu, pk0[3] >> 16});
                    st16_dev(ws, tb + 64, (u32x4){pk1[0] & 0xffffu, pk1[0] >> 16, pk1[1] & 0xffffu, pk1[1] >> 16});
                    st16_dev(ws, tb + 96, (u32x4){pk1[2] & 0xffffu, pk1[2] >> 16, pk1[3] & 0xffffu, pk1[3] >> 16});
                }
            }
            SBAR();
        });
#undef P1_K
#undef P1_T
#undef WAITX
    }
    STAMP(1, 2);
    bf16x8 wb[WD + 1];
    asm volatile("s_waitcnt vmcnt(8) lgkmcnt(0)\n\ts_barrier" ::: "memory");
    {
        const lds_char* p = opaque_lds(c.rb0 + k1_slot(0, 0));
#pragma unroll
        for (int k = 0; k < WD; ++k) wb[k] = wfrag(p, k * 1024, k * 1024 + 256);
    }
    SBAR();
    STAMP(1, 3);
    unsigned peek = 0;
    p_tile<0, 0, true, 1, 1, 5>(c, xf, wb, aQ, aC, 0, 2, peek);
    p_tile<0, 1, true, 0, 2, 1>(c, xf, wb, aV, aD, 1, 0, peek);
    STAMP(1, 4);
    p_tile<1, 0, true, 1, 1, 0>(c, xf, wb, aC, aQ, 2, 3, peek);
    c.hook_cnt = CNT_QKV + attn_head(blockIdx.x);
    p_tile<1, 1, true, 0, 2, 4>(c, xf, wb, aD, aV, 3, 0, peek);
    STAMP(1, 5);
    p_tile<0, 0, true, 2, 1, 2>(c, xf, wb, aQ, aC, 4, 4, peek);
    p_tile<0, 1, true, 0, 2, 0>(c, xf, wb, aV, aD, 5, 0, peek);
    STAMP(1, 6);
    p_tile<1, 0, true, 0, 0, 3>(c, xf, wb, aC, aQ, 6, 0, peek);
    STAMP(1, 7);
    glu_store(c, aV, 7);
    glu_store(c, aC, 8);
}

__device__ __forceinline__ void store_o(const char* ws, int head, int tq, int lane, const f32x16& o) {
    const int r = lane & 31, hh = lane >> 5;
    const auto sw = __builtin_amdgcn_permlane32_swap(__float_as_uint(o[10]), __float_as_uint(o[10]), false, false);
    const float inv = rcp(__uint_as_float(sw[0]));
    u32x4 m = {cvtpk(o[0] * inv, o[1] * inv), cvtpk(o[2] * inv, o[3] * inv), cvtpk(o[4] * inv, o[5] * inv), cvtpk(o[6] * inv, o[7] * inv)};
    st16_dev(ws + WS_OF + (size_t)(tq * NKS + head) * 1024, lane * 16, m);
    if (hh == 0)
        asm volatile("global_store_dword %0, %1, %2 sc1" :: "v"((unsigned)(r * 4)), "v"(cvtpk(o[8] * inv, o[9] * inv)),
                     "s"(ws + WS_OT + (size_t)(tq * NH + head) * 128) : "memory");
}

__device__ __forceinline__ void attn_check_store(char* __restrict__ ws, int head, int tq, int lane, f32x16 fo) {
    const int r = lane & 31, hh = lane >> 5;
    bool bad = false;
    if (hh == 0) bad = !(fo[10] >= 0x1p-60f && fo[10] <= 0x1p60f) || !(fabsf(fo[8]) <= 0x1p120f) || !(fabsf(fo[9]) <= 0x1p120f);
#pragma unroll
    for (int e = 0; e < 8; ++e) bad |= !(fabsf(fo[e]) <= 0x1p120f);
    if (__builtin_expect(__any(bad), 0)) {
        const char* kvg = ws + WS_KV + (size_t)head * NTT * KV_REC;
        const bf16x8 fq = *(const bf16x8*)(ws + WS_QM + ((size_t)(head * NTT + tq) * 64 + lane) * 16);
        const bf16x4 fqt = __builtin_bit_cast(bf16x4, (u32x2){((const unsigned*)(ws + WS_QT))[(size_t)(head * 2 + hh) * S_ + tq * 32 + r], 0u});
        f32x16 acc = {};
        float m = -INFINITY;
#pragma unroll 1
        for (int kt = 0; kt < NTT; ++kt) {
            const char* rec = kvg + (size_t)kt * KV_REC;
            const bf16x8 fk = *(const bf16x8*)(rec + lane * 16);
            const bf16x4 fkt = __builtin_bit_cast(bf16x4, (u32x2){*(const unsigned*)(rec + KV_T + lane * 4), 0u});
            const char* fv = rec + KV_V + hh * 320 + (r < 19 ? r : 19) * 16;
            const bf16x8 f0 = *(const bf16x8*)fv, f1 = *(const bf16x8*)(fv + 640);
            f32x16 s = MFMA32(fk, fq, (f32x16){});
            s = MFMA32K8(fkt, fqt, s);
            float mx = s[0];
#pragma unroll
            for (int e = 1; e < 16; ++e) mx = fmaxf(mx, s[e]);
            mx = fmaxf(mx, __shfl_xor(mx, 32));
            const float mn = fmaxf(m, mx);
            const float al = (mn == -INFINITY) ? 1.0f : ex2(m - mn);
            const float sub = (mn == -INFINITY) ? 0.0f : mn;
            m = mn;
            u32x4 p0, p1;
#pragma unroll
            for (int e = 0; e < 4; ++e) {
                p0[e] = cvtpk(ex2(s[2 * e] - sub), ex2(s[2 * e + 1] - sub));
                p1[e] = cvtpk(ex2(s[8 + 2 * e] - sub), ex2(s[8 + 2 * e + 1] - sub));
            }
#pragma unroll
            for (int e = 0; e < 16; ++e) acc[e] *= al;
            acc = MFMA32(f0, __builtin_bit_cast(bf16x8, p0), acc);
            acc = MFMA32(f1, __builtin_bit_cast(bf16x8, p1), acc);
        }
        fo = acc;
    }
    store_o(ws, head, tq, lane, fo);
}

__device__ __forceinline__ void fn_o_load(const char* ws, int ft, int w, unsigned lo, u32x4 (&ot)[5]) {
    const char* osrc = ws + WS_OF + (size_t)ft * NKS * 1024 + w * 4096;
    const char* osrc5 = ws + WS_OT + (size_t)ft * NH * 128 + (w & 3) * 1024;
    asm volatile("global_load_dwordx4 %0, %5, %6 sc1\n\tglobal_load_dwordx4 %1, %5, %6 offset:1024 sc1\n\t"
                 "global_load_dwordx4 %2, %5, %6 offset:2048 sc1\n\tglobal_load_dwordx4 %3, %5, %6 offset:3072 sc1\n\t"
                 "global_load_dwordx4 %4, %5, %7 sc1"
                 : "=&v"(ot[0]), "=&v"(ot[1]), "=&v"(ot[2]), "=&v"(ot[3]), "=&v"(ot[4]) : "v"(lo), "s"(osrc), "s"(osrc5) : "memory");
}
__device__ __forceinline__ void fn_o_write(char* smem, int w, int lane, const u32x4 (&ot)[5]) {
    u32x4* od = (u32x4*)(smem + FN_O);
#pragma unroll
    for (int i = 0; i < 4; ++i) od[(w * 4 + i) * 64 + lane] = ot[i];
    if (w < 4) {
        const int hl = lane >> 3, t0 = 4 * (lane & 7);
        unsigned* d = (unsigned*)(smem + FN_O + (32 + w) * 1024 + (t0 + 32 * (hl >> 2)) * 16 + (hl & 3) * 4);
#pragma unroll
        for (int e = 0; e < 4; ++e) d[4 * e] = ot[4][e];
    }
}
__device__ __forceinline__ void fn_ldw(bf16x8 (&wf)[NKS], const char* src, unsigned lo, int k0) {
    asm volatile("global_load_dwordx4 %0, %4, %5\n\tglobal_load_dwordx4 %1, %4, %5 offset:1024\n\t"
                 "global_load_dwordx4 %2, %4, %5 offset:2048\n\tglobal_load_dwordx4 %3, %4, %5 offset:3072"
                 : "=&v"(wf[k0]), "=&v"(wf[k0 + 1]), "=&v"(wf[k0 + 2]), "=&v"(wf[k0 + 3]) : "v"(lo), "s"(src + k0 * 1024) : "memory");
}
__device__ __forceinline__ void fn_p_load(const char* ws, int ft, int nt, int sp, unsigned lo, u32x4 (&pq)[3][2]) {
#pragma unroll
    for (int i = 0; i < 3; ++i) {
        const int q = (sp + 1 + i) & 3;
        const char* pb = ws + WS_PD + (size_t)((q * NTT + ft) * 18 + nt) * 2048;
        asm volatile("global_load_dwordx4 %0, %2, %3 sc1\n\tglobal_load_dwordx4 %1, %2, %3 offset:1024 sc1"
                     : "=&v"(pq[i][0]), "=&v"(pq[i][1]) : "v"(lo), "s"(pb) : "memory");
    }
}
__device__ __forceinline__ void fn_gate(const char* smem, int lane, int ti, const u32x4 (&pq)[3][2], float (&gt)[16]) {
    f32x4 mv[4];
#pragma unroll
    for (int e = 0; e < 4; ++e) mv[e] = ((const f32x4*)(smem + FN_OWN))[(ti * 4 + e) * 64 + lane];
#pragma unroll
    for (int hf2 = 0; hf2 < 2; ++hf2)
#pragma unroll
        for (int i = 0; i < 4; ++i) {
            const int e = hf2 * 8 + 2 * i;
            const float dlo = (mv[e >> 2][e & 3] + bflo(pq[0][hf2][i])) + (bflo(pq[1][hf2][i]) + bflo(pq[2][hf2][i]));
            const float dhi = (mv[e >> 2][(e & 3) + 1] + bfhi(pq[0][hf2][i])) + (bfhi(pq[1][hf2][i]) + bfhi(pq[2][hf2][i]));
            gt[e] = dlo * rcp(1.0f + ex2(-LOG2E * dlo)); gt[e + 1] = dhi * rcp(1.0f + ex2(-LOG2E * dhi));
            PIN(gt[e]); PIN(gt[e + 1]);
        }
}
__device__ __forceinline__ void fn_gate_stage(const char* ws, const char* smem, int cnt_p, unsigned peek_p, int ft, int nt, int sp, int ti,
                                              int lane, float (&gt)[16]) {
    u32x4 pq[3][2];
    cnt_wait(ws, cnt_p, peek_p, 4);
    fn_p_load(ws, ft, nt, sp, lane * 16, pq);
    asm volatile("s_waitcnt vmcnt(0)" : "+v"(pq[0][0]), "+v"(pq[0][1]), "+v"(pq[1][0]), "+v"(pq[1][1]), "+v"(pq[2][0]), "+v"(pq[2][1]) :: "memory");
    fn_gate(smem, lane, ti, pq, gt);
}
__device__ __forceinline__ void fn_tile(unsigned lds0, int lane, const bf16x8 (&wf)[NKS], const float (&gt)[16],
                                        u32x4 x0, u32x4 x1, u16* __restrict__ op_) {
    typedef const __attribute__((address_space(3))) bf16x8* lds_frag;
    const lds_char* op = opaque_lds(lds0 + FN_O + lane * 16);
    f32x16 sa = {};
    bf16x8 ofr[2][2];
#pragma unroll
    for (int k = 0; k < 2; ++k) ofr[0][k] = *(lds_frag)(op + k * 1024);
#pragma unroll
    for (int k0 = 0; k0 < NKS; k0 += 2) {
        const int cur = (k0 >> 1) & 1;
        if (k0 + 2 < NKS) {
#pragma unroll
            for (int k = 0; k < 2; ++k) ofr[cur ^ 1][k] = *(lds_frag)(op + (k0 + 2 + k) * 1024);
        }
        SBAR();
#pragma unroll
        for (int k = 0; k < 2; ++k) sa = MFMA32(wf[k0 + k], ofr[cur][k], sa);
        SBAR();
    }
    u32x4 res[2];
#pragma unroll
    for (int hf2 = 0; hf2 < 2; ++hf2)
#pragma unroll
        for (int i = 0; i < 4; ++i) {
            const int e = hf2 * 8 + 2 * i;
            const unsigned xv = hf2 ? x1[i] : x0[i];
            res[hf2][i] = cvtpk(bflo(xv) + sa[e] * gt[e], bfhi(xv) + sa[e + 1] * gt[e + 1]);
        }
    *(u32x4*)(op_) = res[0];
    *(u32x4*)(op_ + 16) = res[1];
}

template <int NT>
__device__ __forceinline__ void role_attn(const u16* __restrict__ x, u16* __restrict__ out, char* __restrict__ ws, char* smem, unsigned lds0,
                                          int cnt_h_mine, int tp, int g, int nt0, int sp) {
    const int tid = fresh_tid(), lane = tid & 63, r = lane & 31, hh = lane >> 5;
    const int w = __builtin_amdgcn_readfirstlane(tid >> 6);
    const int head = attn_head(blockIdx.x), qb = blockIdx.x >> 5;
    const int qg = w & 1, kh = w >> 1;
    const lds_char* lds = (const lds_char*)smem;
    const unsigned lo = lane * 16;
    typedef const __attribute__((address_space(3))) bf16x8* lds_frag;
    typedef const __attribute__((address_space(3))) unsigned* lds_u32;
    asm volatile("s_waitcnt vmcnt(3) lgkmcnt(0)\n\ts_barrier" ::: "memory");
    bf16x8 qm[4]; bf16x4 qt[4];
#pragma unroll
    for (int i = 0; i < 4; ++i) {
        qm[i] = *(lds_frag)(lds + AT_Q + (qg * 4 + i) * 1024 + lane * 16);
        qt[i] = __builtin_bit_cast(bf16x4, (u32x2){*(lds_u32)(lds + AT_Q + 8192 + qg * 1024 + hh * 512 + (i * 32 + r) * 4), 0u});
    }
    f32x16 o[4] = {};
    bf16x8 kmA, kmB, vA0, vA1, vB0, vB1;
    u32x2 ktA = {0, 0}, ktB = {0, 0};
    const lds_char* kp = lds + kh * AT_STAGE + lane * 16;
    const lds_char* tp_ = lds + kh * AT_STAGE + KV_T + lane * 4;
    const lds_char* vp = lds + kh * AT_STAGE + KV_V + hh * 320 + (r < 19 ? r : 19) * 16;
#define LDK(KM, KT, off) do { KM = *(lds_frag)(kp + (off)); KT[0] = *(lds_u32)(tp_ + (off)); } while (0)
#define LDV(V0, V1, off) do { V0 = *(lds_frag)(vp + (off)); V1 = *(lds_frag)(vp + (off) + 640); } while (0)
    float ea = 0.f, eb = 0.f;
    float ec = 0.f, ed = 0.f;
#define EXP4(SC, e0, PD, d0) do { \
        unsigned c0 = cvtpk(ea, eb), c1 = cvtpk(ec, ed); PIN(c0); PIN(c1); PD[d0] = c0; PD[d0 + 1] = c1; \
        ea = ex2(SC[e0]); eb = ex2(SC[e0 + 1]); ec = ex2(SC[e0 + 2]); ed = ex2(SC[e0 + 3]); PIN(ea); PIN(eb); PIN(ec); PIN(ed); } while (0)
#define SLOT(SC, SN, PC0, PC1, PP0, PP1, OP, VP0, VP1, KM, KT, QI) do { \
        SN = MFMA32(KM, qm[QI], (f32x16){});                    PIN(SN); SBAR(); EXP4(SC, 0, PP1, 2);  SBAR(); \
        OP = MFMA32(VP0, __builtin_bit_cast(bf16x8, PP0), OP);  PIN(OP); SBAR(); EXP4(SC, 4, PC0, 0);  SBAR(); \
        SN = MFMA32K8(__builtin_bit_cast(bf16x4, KT), qt[QI], SN); PIN(SN); SBAR(); EXP4(SC, 8, PC0, 2);  SBAR(); \
        OP = MFMA32(VP1, __builtin_bit_cast(bf16x8, PP1), OP);  PIN(OP); SBAR(); EXP4(SC, 12, PC1, 0); SBAR(); } while (0)
#define TILE(KC, TC, KN, TN, VC0, VC1, VO0, VO1, off, MID) do { \
        SLOT(sX, sY, pX0, pX1, pY0, pY1, o[3], VO0, VO1, KC, TC, 1); \
        SLOT(sY, sX, pY0, pY1, pX0, pX1, o[0], VC0, VC1, KC, TC, 2); \
        MID; LDK(KN, TN, off); SBAR(); \
        SLOT(sX, sY, pX0, pX1, pY0, pY1, o[1], VC0, VC1, KC, TC, 3); \
        LDV(VO0, VO1, off); SBAR(); \
        SLOT(sY, sX, pY0, pY1, pX0, pX1, o[2], VC0, VC1, KN, TN, 0); } while (0)
    f32x16 sX, sY;
    u32x4 pX0, pX1, pY0 = {0, 0, 0, 0}, pY1 = {0, 0, 0, 0};
    LDK(kmA, ktA, 0); LDV(vA0, vA1, 0);
    vB0 = vA0; vB1 = vA1;
    sX = MFMA32(kmA, qm[0], (f32x16){});
    sX = MFMA32K8(__builtin_bit_cast(bf16x4, ktA), qt[0], sX);
    STAMP(2, 1);
    auto stage_body = [&](int st, auto first_) __attribute__((always_inline)) {
        constexpr bool FIRST = decltype(first_)::value != 0;
        const int cur = (st & 1) * 2 * AT_STAGE, nxt = cur ^ (2 * AT_STAGE);
        TILE(kmA, ktA, kmB, ktB, vA0, vA1, vB0, vB1, cur + KV_REC, (void)0);
        TILE(kmB, ktB, kmA, ktA, vB0, vB1, vA0, vA1, cur + 2 * KV_REC, (void)0);
        if constexpr (FIRST) {
            asm volatile("s_waitcnt vmcnt(0)\n\ts_barrier" ::: "memory");
            cnt_add(ws, cnt_h_mine);
            SBAR();
        }
        TILE(kmA, ktA, kmB, ktB, vA0, vA1, vB0, vB1, cur + 3 * KV_REC, (void)0);
        TILE(kmB, ktB, kmA, ktA, vB0, vB1, vA0, vA1, nxt,
             { asm volatile("s_waitcnt lgkmcnt(0)\n\ts_barrier" ::: "memory"); SBAR(); });
    };
    stage_body(0, IC<1>{});
#pragma unroll 1
    for (int st = 1; st < 8; ++st) stage_body(st, IC<0>{});
    pY1[2] = cvtpk(ea, eb); pY1[3] = cvtpk(ec, ed);
    o[3] = MFMA32(vB0, __builtin_bit_cast(bf16x8, pY0), o[3]);
    o[3] = MFMA32(vB1, __builtin_bit_cast(bf16x8, pY1), o[3]);
#undef LDK
#undef LDV
#undef EXP4
#undef E2
#undef PK
#undef E2PK
#undef SLOT
#undef TILE
    STAMP(2, 2);
    {
        f32x4* red = (f32x4*)(smem + AT_RED);
#pragma unroll
        for (int t = 0; t < 2; ++t) {
            const f32x16& gq = kh ? o[t] : o[2 + t];
#pragma unroll
            for (int e = 0; e < 3; ++e) red[((w * 2 + t) * 3 + e) * 64 + lane] = (f32x4){gq[4 * e], gq[4 * e + 1], gq[4 * e + 2], gq[4 * e + 3]};
        }
        asm volatile("s_waitcnt lgkmcnt(0)\n\ts_barrier" ::: "memory");
#pragma unroll
        for (int t = 0; t < 2; ++t) {
            f32x16 fo = kh ? o[2 + t] : o[t];
#pragma unroll
            for (int e = 0; e < 3; ++e) {
                const f32x4 p = red[(((w ^ 2) * 2 + t) * 3 + e) * 64 + lane];
#pragma unroll
                for (int i = 0; i < 4; ++i) fo[4 * e + i] += p[i];
            }
            attn_check_store(ws, head, qb * 8 + qg * 4 + 2 * kh + t, lane, fo);
        }
    }
    STAMP(2, 3);
    const int ft = tp * 4 + sp;
    asm volatile("s_waitcnt vmcnt(0)\n\ts_barrier" ::: "memory");
    cnt_add(ws, CNT_O + qb);
    cnt_add(ws, CNT_P + tp * 4 + g);
    u32x4 ot[5];
    if (NT == 5 && w == 0) {
        const int nt = nt0 + 4;
        const size_t xo = (size_t)(ft * 32 + r) * D_ + nt * 32 + 8 * hh;
        bf16x8 wf[NKS]; u32x4 x0, x1; float gt[16]; unsigned pp;
#pragma unroll
        for (int k0 = 0; k0 < NKS; k0 += 4) fn_ldw(wf, ws + WS_WOF + (size_t)nt * NKS * 1024, lo, k0);
        asm volatile("global_load_dwordx4 %0, %2, off\n\tglobal_load_dwordx4 %1, %2, off offset:32" : "=&v"(x0), "=&v"(x1) : "v"(x + xo) : "memory");
        cnt_peek(ws, CNT_P + tp * 4 + g, pp);
        asm volatile("s_waitcnt vmcnt(0)" : "+v"(pp), "+v"(x0), "+v"(x1) :: "memory");
#pragma unroll
        for (int k0 = 0; k0 < NKS; k0 += 4) asm volatile("" : "+v"(wf[k0]), "+v"(wf[k0 + 1]), "+v"(wf[k0 + 2]), "+v"(wf[k0 + 3]));
        fn_gate_stage(ws, smem, CNT_P + tp * 4 + g, pp, ft, nt, sp, 4, lane, gt);
        asm volatile("s_barrier" ::: "memory");
        fn_o_load(ws, ft, w, lo, ot);
        asm volatile("s_waitcnt vmcnt(0)" : "+v"(ot[0]), "+v"(ot[1]), "+v"(ot[2]), "+v"(ot[3]), "+v"(ot[4]) :: "memory");
        fn_o_write(smem, w, lane, ot);
        asm volatile("s_waitcnt lgkmcnt(0)\n\ts_barrier" ::: "memory");
        fn_tile(lds0, lane, wf, gt, x0, x1, out + xo);
    } else {
        asm volatile("s_barrier" ::: "memory");
        fn_o_load(ws, ft, w, lo, ot);
        asm volatile("s_waitcnt vmcnt(0)" : "+v"(ot[0]), "+v"(ot[1]), "+v"(ot[2]), "+v"(ot[3]), "+v"(ot[4]) :: "memory");
        fn_o_write(smem, w, lane, ot);
        asm volatile("s_waitcnt lgkmcnt(0)\n\ts_barrier" ::: "memory");
    }
}

template <int NT>
__device__ __forceinline__ void role_down(const u16* __restrict__ wd, const u16* __restrict__ x, u16* __restrict__ out, char* __restrict__ ws,
                                          char* smem, unsigned lds0, int cnt_h_need, int tp, int g, int nt0, int sp) {
    const int tid = fresh_tid(), lane = tid & 63, r = lane & 31, hh = lane >> 5;
    const int w = __builtin_amdgcn_readfirstlane(tid >> 6), t4 = w - 4;
    const int tt = tp * 4 + t4, ft = tp * 4 + sp;
    const unsigned lo = lane * 16;
    unsigned voff[12];
#pragma unroll
    for (int i = 0; i < 12; ++i) {
        const int c = min(t4 + 4 * i, 44) * 64 + lane, row = c / 20;
        int cc = c - row * 20;
        if (cc >= NT * 4) cc = 0;
        voff[i] = (unsigned)(row * (D_ * 2) + cc * 16);
    }
    const char* wsrc = (const char*)(wd + (size_t)sp * 1152 * D_ + nt0 * 32);
    auto piece = [&](int s, int i) __attribute__((always_inline)) {
        if (i < 11 || t4 == 0) glds16(wsrc + (size_t)s * 144 * (D_ * 2), voff[i], lds0 + DN_RING + (s & 1) * DN_SLAB + (t4 + 4 * i) * 1024);
    };
    const char* kvsrc = ws + WS_KV + ((size_t)attn_head(blockIdx.x) * NTT + (t4 >> 1) * 32) * KV_REC + (t4 & 1) * 5120;
    const unsigned kvdst = lds0 + (t4 >> 1) * AT_STAGE + (t4 & 1) * 5120;
    auto kvpiece = [&](int st, int i) __attribute__((always_inline)) {
        glds16(kvsrc + (size_t)st * AT_STAGE + i * 1024, lo, kvdst + (st & 1) * 2 * AT_STAGE + i * 1024);
    };
    const char* hsrc = ws + WS_H + (size_t)(tt * NKB + sp * 72) * 1024;
    bf16x8 hf[2][9];
    auto hload = [&](bf16x8& d, int kb) __attribute__((always_inline)) {
        const unsigned vo = lo;
        const char* p = hsrc + (size_t)kb * 1024;
        asm volatile("global_load_dwordx4 %0, %1, %2 sc1" : "=&v"(d) : "v"(vo), "s"(p) : "memory");
    };
    const int grp = (lane >> 4) & 1, q4 = (lane & 15) >> 2, p4 = lane & 3;
    const unsigned rp = lds0 + DN_RING + (8 * hh + q4) * 320 + grp * 32 + (((p4 & 1) << 4) | ((p4 >> 1) << 3));

    asm volatile("s_waitcnt vmcnt(3) lgkmcnt(0)\n\ts_barrier" ::: "memory");
#pragma unroll
    for (int i = 0; i < 5; ++i) kvpiece(1, i);
#pragma unroll
    for (int i = 0; i < 12; ++i) piece(0, i);
    asm volatile("s_waitcnt vmcnt(16)\n\ts_barrier" ::: "memory");
    unsigned peek_h;
    cnt_peek(ws, cnt_h_need, peek_h);
    f32x16 acc[NT] = {};
    const int nt = nt0 + t4;
    bf16x8 wf[NKS];
    const char* wsrc2 = ws + WS_WOF + (size_t)nt * NKS * 1024;
#define HPIN(P) "+v"(hf[P][0]), "+v"(hf[P][1]), "+v"(hf[P][2]), "+v"(hf[P][3]), "+v"(hf[P][4]), "+v"(hf[P][5]), "+v"(hf[P][6]), "+v"(hf[P][7]), "+v"(hf[P][8])
    auto slab = [&](int s, auto pc_, auto mc_) __attribute__((always_inline)) {
        constexpr int P = decltype(pc_)::value, MODE = decltype(mc_)::value;
        constexpr int PD = G_PD2, N = 9 * NT;
        if constexpr (MODE == 0) {
            asm volatile("s_waitcnt vmcnt(0) lgkmcnt(0)\n\ts_barrier" : "+v"(peek_h) :: "memory");
            cnt_wait(ws, cnt_h_need, peek_h, 8);
            static_for<0, 9>([&](auto ic) __attribute__((always_inline)) { hload(hf[0][decltype(ic)::value], decltype(ic)::value); });
            asm volatile("s_waitcnt vmcnt(0)" : HPIN(0) :: "memory");
        } else if constexpr (P == 0) asm volatile("s_waitcnt vmcnt(0) lgkmcnt(0)\n\ts_barrier" : HPIN(0) :: "memory");
        else asm volatile("s_waitcnt vmcnt(0) lgkmcnt(0)\n\ts_barrier" : HPIN(1) :: "memory");
        const lds_char* p = opaque_lds(rp + P * DN_SLAB);
        bf16x8 wb[PD + 1];
        auto rdw = [&](auto mc2) __attribute__((always_inline)) {
            constexpr int m = decltype(mc2)::value;
            wb[m % (PD + 1)] = wfrag(p, (m / NT) * 16 * 320 + (m % NT) * 64, (m / NT) * 16 * 320 + (m % NT) * 64 + 4 * 320);
        };
        static_for<0, PD>(rdw);
        static_for<0, N>([&](auto nc) __attribute__((always_inline)) {
            constexpr int n = decltype(nc)::value;
            if constexpr (n + PD < N) rdw(IC<n + PD>{});
            static_for<0, 26>([&](auto ic) __attribute__((always_inline)) {
                constexpr int i = decltype(ic)::value;
                if constexpr ((i * (N - 4)) / 26 == n) {
                    if constexpr (i < 5) { if (s + 2 < 8) kvpiece(s + 2, i); }
                    else if constexpr (i < 14) { if constexpr (MODE < 2) hload(hf[P ^ 1][i - 5], 9 * (s + 1) + i - 5); }
                    else { if constexpr (MODE < 2) piece(s + 1, i - 14); }
                }
            });
            if constexpr (MODE == 2) {
                if constexpr (n % 6 == 0 && n / 6 < 5) fn_ldw(wf, wsrc2, lo, 4 * (n / 6));
                if constexpr (n == E1_POS) asm volatile("s_barrier" ::: "memory");
            }
            acc[n % NT] = MFMA32(wb[n % (PD + 1)], hf[P][n / NT], acc[n % NT]);
            PIN(acc[n % NT]);
            SBAR();
        });
    };
    slab(0, IC<0>{}, IC<0>{});
#pragma unroll 1
    for (int s = 1; s < 7; s += 2) { slab(s, IC<1>{}, IC<1>{}); slab(s + 1, IC<0>{}, IC<1>{}); }
    slab(7, IC<1>{}, IC<2>{});
#undef HPIN
    STAMP(2, 4);
    if (t4 == sp) {
        f32x4* mine = (f32x4*)(smem + FN_OWN);
#pragma unroll
        for (int t = 0; t < NT; ++t)
#pragma unroll
            for (int e = 0; e < 4; ++e) mine[(t * 4 + e) * 64 + lane] = (f32x4){acc[t][4 * e], acc[t][4 * e + 1], acc[t][4 * e + 2], acc[t][4 * e + 3]};
    } else {
#pragma unroll
        for (int t = 0; t < NT; ++t) {
            const char* pb = ws + WS_PD + (size_t)((sp * NTT + tt) * 18 + nt0 + t) * 2048;
            st16_dev(pb, lo, (u32x4){cvtpk(acc[t][0], acc[t][1]), cvtpk(acc[t][2], acc[t][3]), cvtpk(acc[t][4], acc[t][5]), cvtpk(acc[t][6], acc[t][7])});
            st16_dev(pb + 1024, lo, (u32x4){cvtpk(acc[t][8], acc[t][9]), cvtpk(acc[t][10], acc[t][11]), cvtpk(acc[t][12], acc[t][13]), cvtpk(acc[t][14], acc[t][15])});
        }
    }
    STAMP(2, 5);
    const size_t xo = (size_t)(ft * 32 + r) * D_ + nt * 32 + 8 * hh;
    u32x4 x0, x1, ot[5];
    float gt[16];
    fn_ldw(wf, wsrc2, lo, 20); fn_ldw(wf, wsrc2, lo, 24);
    asm volatile("s_waitcnt vmcnt(8) lgkmcnt(0)\n\ts_barrier" ::: "memory");
    fn_ldw(wf, wsrc2, lo, 28); fn_ldw(wf, wsrc2, lo, 32);
    asm volatile("global_load_dwordx4 %0, %2, off\n\tglobal_load_dwordx4 %1, %2, off offset:32" : "=&v"(x0), "=&v"(x1) : "v"(x + xo) : "memory");
    STAMP(2, 6);
    {
        unsigned po, pp;
        cnt_peek(ws, CNT_P + tp * 4 + g, pp);
        asm volatile("s_waitcnt vmcnt(0)" : "+v"(pp), "+v"(x0), "+v"(x1) :: "memory");
#pragma unroll
        for (int k0 = 0; k0 < NKS; k0 += 4) asm volatile("" : "+v"(wf[k0]), "+v"(wf[k0 + 1]), "+v"(wf[k0 + 2]), "+v"(wf[k0 + 3]));
        fn_gate_stage(ws, smem, CNT_P + tp * 4 + g, pp, ft, nt, sp, t4, lane, gt);
        cnt_peek(ws, CNT_O + (ft >> 3), po);
        asm volatile("s_waitcnt vmcnt(0)" : "+v"(po) :: "memory");
        if (w == 4) cnt_wait(ws, CNT_O + (ft >> 3), po, NH);
    }
    asm volatile("s_barrier" ::: "memory");
    STAMP(2, 8);
    fn_o_load(ws, ft, w, lo, ot);
    asm volatile("s_waitcnt vmcnt(0)" : "+v"(ot[0]), "+v"(ot[1]), "+v"(ot[2]), "+v"(ot[3]), "+v"(ot[4]) :: "memory");
    fn_o_write(smem, w, lane, ot);
    asm volatile("s_waitcnt lgkmcnt(0)\n\ts_barrier" ::: "memory");
    STAMP(2, 7);
    fn_tile(lds0, lane, wf, gt, x0, x1, out + xo);
    STAMP(2, 9);
}

__global__ __launch_bounds__(512, 2) void k1_main(const u16* __restrict__ wg, const u16* __restrict__ wu, const u16* __restrict__ wd,
                                                  const u16* __restrict__ x, u16* __restrict__ out, char* __restrict__ ws) {
    extern __shared__ __attribute__((aligned(16))) char smem[];
    const unsigned lds0 = (unsigned)(uintptr_t)(const lds_char*)smem;
    const int xcd = blockIdx.x & 7, idx = blockIdx.x >> 3;
    STAMP(1, 0);
    phase_proj(wg, wu, ws, lds0);
    STAMP(1, 8);
    const int sp = xcd >> 1, tp = (xcd & 1) * 8 + (idx & 7), g = idx >> 3;
    const int panel_mine = (xcd & 1) * 4 + (idx & 3), cq_mine = xcd >> 1;
    const int nt0 = g == 0 ? 0 : g == 1 ? 5 : g == 2 ? 9 : 14;
    const int cnt_h_mine = CNT_H + panel_mine * 4 + cq_mine, cnt_h_need = CNT_H + (tp >> 1) * 4 + sp;
    if (__builtin_amdgcn_readfirstlane(threadIdx.x) < 256) {
        if (g & 1) role_attn<4>(x, out, ws, smem, lds0, cnt_h_mine, tp, g, nt0, sp);
        else role_attn<5>(x, out, ws, smem, lds0, cnt_h_mine, tp, g, nt0, sp);
    } else {
        if (g & 1) role_down<4>(wd, x, out, ws, smem, lds0, cnt_h_need, tp, g, nt0, sp);
        else role_down<5>(wd, x, out, ws, smem, lds0, cnt_h_need, tp, g, nt0, sp);
    }
}

extern "C" void kernel_launch(void* const* d_in, const int* in_sizes, int n_in, void* d_out, int out_size, void* d_ws, size_t ws_size,
                              hipStream_t stream) {
    const u16* x = (const u16*)d_in[0];
    const u16* wqkv = (const u16*)d_in[1];
    const u16* wo = (const u16*)d_in[2];
    const u16* wg = (const u16*)d_in[4];
    const u16* wu = (const u16*)d_in[5];
    const u16* wd = (const u16*)d_in[6];
    char* ws = (char*)d_ws;
    (void)hipFuncSetAttribute((const void*)k1_main, hipFuncAttributeMaxDynamicSharedMemorySize, K1_LDS);
    k0_prep<<<256, K0_THREADS, 0, stream>>>(x, wqkv, wo, wg, wu, ws);
    k1_main<<<256, 512, K1_LDS, stream>>>(wg, wu, wd, x, (u16*)d_out, ws);
}
```
